```python
import torch

def _softmax(x, axis=-1):
    return torch.softmax(x, dim=axis)

def _gelu(x):
    return torch.nn.functional.gelu(x, approximate='tanh')

def _silu(x):
    return torch.nn.functional.silu(x)

def _sigmoid(x):
    return torch.sigmoid(x)

def _layernorm(x, axis=-1, eps=1e-05):
    m = x.mean(dim=axis, keepdim=True)
    v = x.var(dim=axis, keepdim=True, unbiased=False)
    return (x - m) / torch.sqrt(v + eps)

def _scatter_add(tgt, idx, src, axis=0):
    return tgt.clone().index_add_(axis, idx, src)

def _segment_sum(src, seg, n):
    out = torch.zeros((n,) + src.shape[1:], dtype=src.dtype)
    return out.index_add_(0, seg, src)

def _pad(x, pw):
    flat = tuple((v for pair in reversed(pw) for v in pair))
    return torch.nn.functional.pad(x, flat)
kernel_name = 'parallel_sum__memory_aug_recurrent_mem_tr__dense_activation_mish__prompt'

def setup_inputs():

    _w = lambda a: (lambda t: t.to(torch.bfloat16) if t.is_floating_point() else t)(torch.as_tensor(a))

    def _A_setup():
        B, S, Dm = (1, 2048, 1024)
        H, Dh = (32, 128)
        M, seg = (8, 32)
        seg = min(seg, S)
        S = S // seg * seg
        return {'x': _w((lambda *s: torch.randn(*s, dtype=torch.bfloat16) * 0.05)(B, S, Dm)), 'mem0': _w((lambda *s: torch.randn(*s, dtype=torch.bfloat16) * 0.2)(B, M, Dm)), 'Wq': _w((lambda *s: torch.randn(*s, dtype=torch.bfloat16) * 1.6)(Dm, H, Dh) * Dm ** (-0.5)), 'Wk': _w((lambda *s: torch.randn(*s, dtype=torch.bfloat16) * 1.6)(Dm, H, Dh) * Dm ** (-0.5)), 'Wv': _w((lambda *s: torch.randn(*s, dtype=torch.bfloat16) * 0.2)(Dm, H, Dh) * Dm ** (-0.5)), 'Wo': _w((lambda *s: torch.randn(*s, dtype=torch.bfloat16) * 0.2)(H, Dh, Dm) * (H * Dh) ** (-0.5)), 'seg_len': seg}
    _A_in = _A_setup()

    def _B_setup():
        B, S, D, F = (1, 2048, 1024, 3584)
        return {'x': _w((lambda *s: torch.randn(*s, dtype=torch.bfloat16) * 0.05)(B, S, D)), 'w1': _w((lambda *s: torch.randn(*s, dtype=torch.bfloat16) * 0.4)(D, F) * D ** (-0.5)), 'b1': _w((lambda *s: torch.randn(*s, dtype=torch.bfloat16) * 0.1)(F)), 'w2': _w((lambda *s: torch.randn(*s, dtype=torch.bfloat16) * 0.1)(F, D) * F ** (-0.5))}
    _B_in = _B_setup()
    return {'A_x': _A_in['x'], 'A_mem0': _A_in['mem0'], 'A_Wq': _A_in['Wq'], 'A_Wk': _A_in['Wk'], 'A_Wv': _A_in['Wv'], 'A_Wo': _A_in['Wo'], 'A_seg_len': _A_in['seg_len'], 'B_x': _B_in['x'], 'B_w1': _B_in['w1'], 'B_b1': _B_in['b1'], 'B_w2': _B_in['w2']}

def reference(A_x, A_mem0, A_Wq, A_Wk, A_Wv, A_Wo, A_seg_len, B_x, B_w1, B_b1, B_w2):

    def _A_ref(x, mem0, Wq, Wk, Wv, Wo, seg_len):
        B, S, Dm = x.shape
        H, Dh = (Wq.shape[1], Wq.shape[2])
        M = mem0.shape[1]
        scale = Dh ** (-0.5)
        n_seg = S // seg_len
        L = M + seg_len
        idx = torch.arange(L)
        mask = (idx[:, None] < M) | (idx[:, None] >= idx[None, :])
        mem = mem0
        outs = []
        for s in range(n_seg):
            xs = x[:, s * seg_len:(s + 1) * seg_len, :].clone()
            h = torch.concatenate([mem, xs], axis=1)
            q = torch.einsum('btd,dhe->bhte', h, Wq)
            k = torch.einsum('btd,dhe->bhte', h, Wk)
            v = torch.einsum('btd,dhe->bhte', h, Wv)
            scores = torch.einsum('bhqe,bhke->bhqk', q, k) * scale
            scores = torch.where(mask[None, None], scores, -1000000000.0)
            p = _softmax(scores, axis=-1)
            o = torch.einsum('bhqk,bhke->bhqe', p, v)
            o = torch.einsum('bhte,hed->btd', o, Wo)
            mem = o[:, :M, :].clone()
            outs.append(o[:, M:, :])
        return torch.concatenate(outs, axis=1)

    def _B_ref(x, w1, b1, w2):
        h = torch.einsum('bsd,df->bsf', x, w1) + b1
        sp = torch.log1p(torch.exp(-torch.abs(h))) + torch.where(h > 0, h, torch.zeros_like(h))
        act = h * torch.tanh(sp)
        return torch.einsum('bsf,fd->bsd', act, w2)
    x = A_x
    xn = _layernorm(x, axis=-1)
    return x + _A_ref(xn, A_mem0, A_Wq, A_Wk, A_Wv, A_Wo, A_seg_len) + _B_ref(xn, B_w1, B_b1, B_w2)
```

```cpp
#include <hip/hip_runtime.h>
#include <hip/hip_bf16.h>
#include <stdint.h>

typedef unsigned int u32;
typedef unsigned short u16;
typedef unsigned long long u64;
typedef __attribute__((ext_vector_type(4))) u32 u32x4;
typedef __attribute__((ext_vector_type(2))) u32 u32x2;
typedef __attribute__((ext_vector_type(4))) float f32x4;
typedef __attribute__((ext_vector_type(2))) float f32x2;
typedef __attribute__((ext_vector_type(16))) float f32x16;
typedef __attribute__((ext_vector_type(8))) __bf16 bf16x8;
typedef __attribute__((ext_vector_type(4))) short s16x4;
typedef __attribute__((ext_vector_type(8))) short s16x8;
typedef __attribute__((address_space(3))) s16x4* lds_s16x4;

#define DEVINL __device__ __forceinline__

constexpr int S_ = 2048, D_ = 1024, H_ = 32, DH = 128, HD = 4096, F_ = 3584, NSEG = 64;
constexpr u32 WS_XN = 0;
constexpr u32 WS_QX = WS_XN + (4u << 20);
constexpr u32 WS_KX = WS_QX + (16u << 20);
constexpr u32 WS_VT = WS_KX + (16u << 20);
constexpr u32 WS_ACT = WS_VT + (16u << 20);
constexpr u32 WS_Y = WS_ACT + (14u << 20);
constexpr u32 WS_OX = WS_Y + (8u << 20);
constexpr u32 WS_KM = WS_OX + (16u << 20);
constexpr u32 WS_VM = WS_KM + (4u << 20);
constexpr u32 WS_MB = WS_VM + (4u << 20);
constexpr u32 WS_OB = WS_MB + (1u << 20);
constexpr u32 WS_SB = WS_OB + (4u << 20);
constexpr u32 WS_CT = WS_SB + (10u << 20);
constexpr u32 CT_BYTES = 128u << 10;
constexpr u32 WS_END = WS_CT + CT_BYTES;
constexpr int CT_TICKET = 0, CT_TMO = 32, CT_KV = 64, CT_Q = CT_KV + 8 * 32, CT_F1 = CT_Q + 8 * 32, CT_F2 = CT_F1 + 8 * 32,
              CT_OX = CT_F2 + 8 * 32, CT_MF = 4096;
constexpr u32 SENT = 0xFFFFFFFFu;
constexpr u32 SPIN_CAP = 1u << 18;

struct Params {
  const u16 *x, *mem0, *wq, *wk, *wv, *wo, *w1, *b1, *w2;
  u16* out;
  char* ws;
};

DEVINL float bf2f(u16 v) { return __uint_as_float((u32)v << 16); }
DEVINL float bflo(u32 v) { return __uint_as_float(v << 16); }
DEVINL float bfhi(u32 v) { return __uint_as_float(v & 0xFFFF0000u); }
DEVINL u32 cvtpk(float lo, float hi) {
  typedef __attribute__((ext_vector_type(2))) __bf16 bf16x2;
  bf16x2 r = {(__bf16)lo, (__bf16)hi};
  return __builtin_bit_cast(u32, r);
}
DEVINL float rbf(float v) { return bflo(cvtpk(v, v)); }
DEVINL u32 nosent(u32 v) { return v == SENT ? 0x7FFF7FFFu : v; }
DEVINL f32x4 mfma16(u32x4 a, u32x4 b, f32x4 c) {
  return __builtin_amdgcn_mfma_f32_16x16x32_bf16(__builtin_bit_cast(bf16x8, a), __builtin_bit_cast(bf16x8, b), c, 0, 0, 0);
}
DEVINL f32x4 mfma16k16(u32x2 a, u32x2 b, f32x4 c) {
  return mfma16(u32x4{a[0], a[1], 0, 0}, u32x4{b[0], b[1], 0, 0}, c);
}
DEVINL f32x16 mfma32(u32x4 a, u32x4 b, f32x16 c) {
  return __builtin_amdgcn_mfma_f32_32x32x16_bf16(__builtin_bit_cast(bf16x8, a), __builtin_bit_cast(bf16x8, b), c, 0, 0, 0);
}
DEVINL float xg_max(float x) {
  auto s = __builtin_amdgcn_permlane16_swap(__float_as_uint(x), __float_as_uint(x), false, false);
  x = fmaxf(__uint_as_float(s[0]), __uint_as_float(s[1]));
  auto t = __builtin_amdgcn_permlane32_swap(__float_as_uint(x), __float_as_uint(x), false, false);
  return fmaxf(__uint_as_float(t[0]), __uint_as_float(t[1]));
}
DEVINL float xg_sum(float x) {
  auto s = __builtin_amdgcn_permlane16_swap(__float_as_uint(x), __float_as_uint(x), false, false);
  x = __uint_as_float(s[0]) + __uint_as_float(s[1]);
  auto t = __builtin_amdgcn_permlane32_swap(__float_as_uint(x), __float_as_uint(x), false, false);
  return __uint_as_float(t[0]) + __uint_as_float(t[1]);
}
typedef __amdgpu_buffer_rsrc_t rsrc_t;
DEVINL rsrc_t mkrsrc(const void* p, u32 n) { return __builtin_amdgcn_make_buffer_rsrc((void*)p, 0, n, 0x00020000); }
constexpr int SC1 = 16;
DEVINL u32x4 ld128(rsrc_t r, u32 vo, u32 so) { return __builtin_amdgcn_raw_buffer_load_b128(r, vo, so, SC1); }
DEVINL u32x2 ld64(rsrc_t r, u32 vo, u32 so) { return __builtin_amdgcn_raw_buffer_load_b64(r, vo, so, SC1); }
DEVINL u32 ld32(rsrc_t r, u32 vo, u32 so) { return __builtin_amdgcn_raw_buffer_load_b32(r, vo, so, SC1); }
DEVINL void st128(rsrc_t r, u32x4 v, u32 vo, u32 so) { __builtin_amdgcn_raw_buffer_store_b128(v, r, vo + so, 0, SC1); }
DEVINL void st64(rsrc_t r, u32x2 v, u32 vo, u32 so) { __builtin_amdgcn_raw_buffer_store_b64(v, r, vo, so, SC1); }
DEVINL void st32(rsrc_t r, u32 v, u32 vo, u32 so) { __builtin_amdgcn_raw_buffer_store_b32(v, r, vo, so, SC1); }
#define CBAR() asm volatile("" ::: "memory")
#define VMCNT0() asm volatile("s_waitcnt vmcnt(0)" ::: "memory")
#define BAR() do { asm volatile("s_waitcnt lgkmcnt(0)" ::: "memory"); __builtin_amdgcn_s_barrier(); CBAR(); } while (0)
DEVINL bool isnan4(f32x4 v) { float s = (v[0] + v[1]) + (v[2] + v[3]); return s != s; }
DEVINL bool hassent(u32x4 v) { return (v[0] == SENT) | (v[1] == SENT) | (v[2] == SENT) | (v[3] == SENT); }
DEVINL void nap(int n) { for (int i = 0; i < n; i++) __builtin_amdgcn_s_sleep(1); }

constexpr u32 OOB = 0x7FFFFFF0u;
DEVINL u32 lane0off(u32 off) { return (threadIdx.x & 63) == 0 ? off : OOB; }
DEVINL u32 uni(u32 v) { return (u32)__builtin_amdgcn_readfirstlane((int)v); }
DEVINL void wait_ctr(rsrc_t rw, int word, u32 target) {
  u32 spins = 0;
  for (;;) {
    CBAR();
    if (uni(ld32(rw, WS_CT + word * 4, 0)) >= target) break;
    if ((++spins & 255) == 0) {
      const u32 t = uni(ld32(rw, WS_CT + CT_TMO * 4, 0));
      if (t != 0 || spins > SPIN_CAP) { st32(rw, 1u, lane0off(WS_CT + CT_TMO * 4), 0); break; }
    }
    __builtin_amdgcn_s_sleep(4);
  }
}

DEVINL bool spin_fail(rsrc_t rw, u32& spins) {
  if ((++spins & 63) != 0) return false;
  const u32 t = ld32(rw, WS_CT + CT_TMO * 4, 0);
  if (uni(t) == 0 && spins < SPIN_CAP) return false;
  st32(rw, 1u, lane0off(WS_CT + CT_TMO * 4), 0);
  return true;
}

__global__ __launch_bounds__(256) void prep_kernel(Params p) {
  const int tid = threadIdx.x, lane = tid & 63, w = tid >> 6;
  const int row = blockIdx.x * 4 + w;
  const u32x4* xr = (const u32x4*)(p.x + (size_t)row * D_);
  u32x4 a = xr[lane], b = xr[64 + lane];
  float v[16];
#pragma unroll
  for (int i = 0; i < 4; i++) { v[2 * i] = bflo(a[i]); v[2 * i + 1] = bfhi(a[i]); v[8 + 2 * i] = bflo(b[i]); v[9 + 2 * i] = bfhi(b[i]); }
  float s = 0.f;
#pragma unroll
  for (int i = 0; i < 16; i++) s += v[i];
#pragma unroll
  for (int o = 32; o > 0; o >>= 1) s += __shfl_xor(s, o);
  const float mean = s * (1.0f / D_);
  float q = 0.f;
#pragma unroll
  for (int i = 0; i < 16; i++) { float d = v[i] - mean; q += d * d; }
#pragma unroll
  for (int o = 32; o > 0; o >>= 1) q += __shfl_xor(q, o);
  const float mb = rbf(mean), den = rbf(sqrtf(rbf(rbf(q * (1.0f / D_)) + 1e-5f)));
  u32x4 oa, ob;
#pragma unroll
  for (int i = 0; i < 4; i++) {
    oa[i] = cvtpk(rbf(v[2 * i] - mb) / den, rbf(v[2 * i + 1] - mb) / den);
    ob[i] = cvtpk(rbf(v[8 + 2 * i] - mb) / den, rbf(v[9 + 2 * i] - mb) / den);
  }
  u32x4* xn = (u32x4*)(p.ws + WS_XN + (size_t)row * D_ * 2);
  xn[lane] = oa; xn[64 + lane] = ob;
  const u32 n16 = (WS_CT - WS_MB - 16384) / 16;
  u32x4* pz = (u32x4*)(p.ws + WS_MB + 16384);
  const u32x4 sv = {SENT, SENT, SENT, SENT};
  for (u32 i = blockIdx.x * 256 + tid; i < n16; i += gridDim.x * 256) pz[i] = sv;
  u32x4* cz = (u32x4*)(p.ws + WS_CT);
  const u32x4 z = {0, 0, 0, 0};
  for (u32 i = blockIdx.x * 256 + tid; i < CT_BYTES / 16; i += gridDim.x * 256) cz[i] = z;
  if (blockIdx.x < 4) {
    int i = blockIdx.x * 256 + tid;
    int ko = i >> 3, r = i & 7;
    ((u32x4*)(p.ws + WS_MB))[i] = *(const u32x4*)(p.mem0 + r * D_ + ko * 8);
  }
}

constexpr int L_QK = 0, L_V = 8192, L_O = 16384, L_KV = 20480;
#define D_E1 10
#define D_E2 4
#define D_E3 8

DEVINL u32x4 gather8(const u16* p, int stride) {
  u32x4 r;
#pragma unroll
  for (int j = 0; j < 4; j++) r[j] = (u32)p[(2 * j) * stride] | ((u32)p[(2 * j + 1) * stride] << 16);
  return r;
}

DEVINL void rec_prefetch(rsrc_t rw, char* smem, int s, int h, int c, int lane) {
  const int g = lane >> 4, m = lane & 15;
  if ((s & 7) == 0) { wait_ctr(rw, CT_KV + (s >> 3) * 32, 64); CBAR(); }
  u32x4 k0 = ld128(rw, ((32 * s + m) * HD + h * DH + c * 32 + 8 * g) * 2, WS_KX);
  u32x4 k1 = ld128(rw, ((32 * s + 16 + m) * HD + h * DH + c * 32 + 8 * g) * 2, WS_KX);
  u32x4 v[2];
#pragma unroll
  for (int T = 0; T < 2; T++) {
    u32 o = (((s * 32 + h) * 128 + c * 32 + 16 * T + m) * 32 + 4 * g) * 2;
    u32x2 lo = ld64(rw, o, WS_VT), hi = ld64(rw, o + 32, WS_VT);
    v[T] = u32x4{lo[0], lo[1], hi[0], hi[1]};
  }
  u32x4* dst = (u32x4*)(smem + L_KV + (s & 1) * 4096);
  dst[lane] = k0; dst[64 + lane] = k1; dst[128 + lane] = v[0]; dst[192 + lane] = v[1];
}

DEVINL void rec_role(const Params& p, char* smem, int r) {
  const int tid = threadIdx.x, lane0 = tid & 63;
  const int w = __builtin_amdgcn_readfirstlane(tid >> 6);
  const int h = (r & 7) + 8 * (r >> 5), c = (r >> 3) & 3, jc = (r & 7) * 16 + (r >> 3);
  const int nh = w >> 2, kq = w & 3;
  const rsrc_t rw = mkrsrc(p.ws, WS_END);
  u32x4 wqk[2][8], wv[8], wo[8];
  {
    const int g = lane0 >> 4, n = lane0 & 15;
    const u16* W = nh ? p.wk : p.wq;
#pragma unroll
    for (int t = 0; t < 8; t++) {
      const size_t kr = (size_t)(256 * kq + 32 * t + 8 * g) * HD + h * DH + c * 32 + n;
      wqk[0][t] = gather8(W + kr, HD);
      wqk[1][t] = gather8(W + kr + 16, HD);
      wv[t] = gather8(p.wv + kr + 16 * nh, HD);
      wo[t] = gather8(p.wo + (size_t)(512 * w + 256 * (n >> 3) + 32 * t + 8 * g) * D_ + 8 * jc + (n & 7), D_);
      if (t & 1) {
        asm volatile("" : "+v"(wqk[0][t]), "+v"(wqk[1][t]), "+v"(wv[t]), "+v"(wo[t]), "+v"(wqk[0][t - 1]), "+v"(wqk[1][t - 1]), "+v"(wv[t - 1]), "+v"(wo[t - 1]));
        __builtin_amdgcn_sched_barrier(0);
      }
    }
  }
  if (w == 7) rec_prefetch(rw, smem, 0, h, c, lane0);
  BAR();
  const float scale = 0.08838834764831845f;
  const float l2e = 1.4426950408889634f;

  for (int s = 0; s < NSEG; s++) {
    int lane = lane0;
    asm volatile("" : "+v"(lane));
    const int g = lane >> 4, n = lane & 15;
    u32x4 mf[8];
    f32x4 d0, d1;
    {
      const u32 vo = (32 * kq + g) * 128 + (n & 7) * 16, so = WS_MB + s * 16384;
      u32 spins = 0;
      if (s > 0) nap(D_E1);
      for (;;) {
        CBAR();
#pragma unroll
        for (int t = 0; t < 8; t++) mf[t] = ld128(rw, vo + t * 512, so);
        d0 = f32x4{0, 0, 0, 0}; d1 = d0;
#pragma unroll
        for (int t = 0; t < 8; t++) { d0 = mfma16(wqk[0][t], mf[t], d0); d1 = mfma16(wqk[1][t], mf[t], d1); }
        if (!__any(isnan4(d0 + d1))) break;
        bool miss = false;
#pragma unroll
        for (int t = 0; t < 8; t++) miss |= hassent(mf[t]);
        if (!__any(miss)) break;
        if (spin_fail(rw, spins)) break;
      }
    }
    {
      f32x4 dv = n < 8 ? d0 : d1;
      *(f32x4*)(smem + L_QK + kq * 2048 + nh * 1024 + (n >> 3) * 512 + g * 128 + (n & 7) * 16) = dv;
    }
    BAR();
    u32x4 F;
    if (w == 0) {
      const char* b = smem + L_QK + (n >> 3) * 1024 + (g >> 1) * 512 + (g & 1) * 256 + (n & 7) * 16;
      f32x4 a0 = *(const f32x4*)(b), a1 = *(const f32x4*)(b + 128);
#pragma unroll
      for (int k = 1; k < 4; k++) { a0 += *(const f32x4*)(b + k * 2048); a1 += *(const f32x4*)(b + k * 2048 + 128); }
      F = u32x4{cvtpk(a0[0], a0[1]), cvtpk(a0[2], a0[3]), cvtpk(a1[0], a1[1]), cvtpk(a1[2], a1[3])};
      if (s < NSEG - 1) {
        const u32x4* kv = (const u32x4*)(smem + L_KV + (s & 1) * 4096);
        const f32x4 z = {0, 0, 0, 0};
        f32x4 sx0 = mfma16(kv[lane], F, z), sx1 = mfma16(kv[64 + lane], F, z), sm = mfma16(F, F, z);
        const u32 so = WS_SB + (s * 128 + r) * 1280;
        u32x4 u0, u1, u2;
#pragma unroll
        for (int i = 0; i < 4; i++) {
          u0[i] = nosent(__float_as_uint(sx0[i])); u1[i] = nosent(__float_as_uint(sx1[i])); u2[i] = nosent(__float_as_uint(sm[i]));
        }
        if (n < 8) {
          st128(rw, u0, n * 64 + g * 16, so);
          st128(rw, u1, 512 + n * 64 + g * 16, so);
          if (g >= 2) st128(rw, u2, 1024 + n * 32 + (g - 2) * 16, so);
        }
      }
    }
    {
      f32x4 dv = {0, 0, 0, 0};
#pragma unroll
      for (int t = 0; t < 8; t++) dv = mfma16(mf[t], wv[t], dv);
      *(f32x4*)(smem + L_V + kq * 2048 + nh * 1024 + lane * 16) = dv;
    }
    BAR();
    if (w == 0) {
      u32x2 vm[2];
#pragma unroll
      for (int T = 0; T < 2; T++) {
        const char* b = smem + L_V + T * 1024 + lane * 16;
        f32x4 a = *(const f32x4*)(b);
#pragma unroll
        for (int k = 1; k < 4; k++) a += *(const f32x4*)(b + k * 2048);
        vm[T] = u32x2{cvtpk(a[0], a[1]), cvtpk(a[2], a[3])};
      }
      if (n >= 8) st128(rw, F, (((s * 32 + h) * 8 + (n - 8)) * 128 + c * 32 + 8 * g) * 2, WS_KM);
      if (g >= 2) {
#pragma unroll
        for (int T = 0; T < 2; T++) st64(rw, vm[T], ((s * 32 + h) * 128 + c * 32 + 16 * T + n) * 16 + (g - 2) * 8, WS_VM);
      }
      if (s == NSEG - 1) {
        VMCNT0();
        st32(rw, 1u, lane0off(WS_CT + (CT_MF + (s * 32 + h) * 4 + c) * 4), 0);
      } else {
        const u32x4* kv = (const u32x4*)(smem + L_KV + (s & 1) * 4096);
        const u32x4 vx0 = kv[128 + lane], vx1 = kv[192 + lane];
        f32x4 sx0, sx1, sm;
        const int r0 = (r & 7) + 8 * (4 * (h >> 3));
        const u32 vo = (n & 7) * 64 + g * 16, vm_o = 1024 + (n & 7) * 32 + (g & 1) * 16;
        u32 spins = 0;
        nap(D_E2);
        for (;;) {
          CBAR();
          u32x4 a[4], b[4], e[4];
#pragma unroll
          for (int k = 0; k < 4; k++) {
            const u32 so = WS_SB + (s * 128 + r0 + 8 * k) * 1280;
            a[k] = ld128(rw, vo, so); b[k] = ld128(rw, vo + 512, so); e[k] = ld128(rw, vm_o, so);
          }
          sx0 = (__builtin_bit_cast(f32x4, a[0]) + __builtin_bit_cast(f32x4, a[1])) + (__builtin_bit_cast(f32x4, a[2]) + __builtin_bit_cast(f32x4, a[3]));
          sx1 = (__builtin_bit_cast(f32x4, b[0]) + __builtin_bit_cast(f32x4, b[1])) + (__builtin_bit_cast(f32x4, b[2]) + __builtin_bit_cast(f32x4, b[3]));
          sm = (__builtin_bit_cast(f32x4, e[0]) + __builtin_bit_cast(f32x4, e[1])) + (__builtin_bit_cast(f32x4, e[2]) + __builtin_bit_cast(f32x4, e[3]));
          if (!__any(isnan4(sx0 + sx1 + sm))) break;
          bool miss = false;
#pragma unroll
          for (int k = 0; k < 4; k++) miss |= hassent(a[k]) | hassent(b[k]) | hassent(e[k]);
          if (!__any(miss)) break;
          if (spin_fail(rw, spins)) break;
        }
        VMCNT0();
        st32(rw, 1u, lane0off(WS_CT + (CT_MF + (s * 32 + h) * 4 + c) * 4), 0);
        const float sc = scale * l2e;
        float mx = fmaxf(fmaxf(fmaxf(sx0[0], sx0[1]), fmaxf(sx0[2], sx0[3])), fmaxf(fmaxf(sx1[0], sx1[1]), fmaxf(sx1[2], sx1[3])));
        mx = fmaxf(mx, fmaxf(fmaxf(sm[0], sm[1]), fmaxf(sm[2], sm[3])));
        mx = xg_max(mx);
        const float mo = mx * sc;
        float sum = 0.f;
#pragma unroll
        for (int i = 0; i < 4; i++) {
          sx0[i] = __builtin_amdgcn_exp2f(sx0[i] * sc - mo);
          sx1[i] = __builtin_amdgcn_exp2f(sx1[i] * sc - mo);
          sm[i] = g >= 2 ? __builtin_amdgcn_exp2f(sm[i] * sc - mo) : 0.f;
          sum += sx0[i] + sx1[i] + sm[i];
        }
        sum = xg_sum(sum);
        const float inv = 1.0f / sum;
        const u32x4 px = {cvtpk(sx0[0] * inv, sx0[1] * inv), cvtpk(sx0[2] * inv, sx0[3] * inv), cvtpk(sx1[0] * inv, sx1[1] * inv), cvtpk(sx1[2] * inv, sx1[3] * inv)};
        const u32x2 pm = {cvtpk(sm[0] * inv, sm[1] * inv), cvtpk(sm[2] * inv, sm[3] * inv)};
        const u32x2 z2 = {0, 0};
        const f32x4 z = {0, 0, 0, 0};
        f32x4 o0 = mfma16k16(g >= 2 ? vm[0] : z2, pm, mfma16(vx0, px, z));
        f32x4 o1 = mfma16k16(g >= 2 ? vm[1] : z2, pm, mfma16(vx1, px, z));
        f32x4 oo = n < 8 ? o0 : o1;
        const u32x2 ov = {nosent(cvtpk(oo[0], oo[1])), nosent(cvtpk(oo[2], oo[3]))};
        const int oct = 2 * (n >> 3) + (g >> 1);
        const u32 line = (((h >> 2) * 8 + (h & 1) * 4 + c) * 4 + oct) * 256 + ((h >> 1) & 1) * 128;
        st64(rw, ov, line + (n & 7) * 16 + (g & 1) * 8, WS_OB + s * 65536);
      }
    } else if (w == 7) {
      if (s + 1 < NSEG - 1) rec_prefetch(rw, smem, s + 1, h, c, lane);
    }
    if (s == NSEG - 1) break;
    BAR();
    {
      f32x4 d;
      const u32 vo = lane * 16, so = WS_OB + s * 65536 + w * 8192;
      u32 spins = 0;
      nap(D_E3);
      for (;;) {
        CBAR();
        u32x4 of[8];
#pragma unroll
        for (int t = 0; t < 8; t++) of[t] = ld128(rw, vo + t * 1024, so);
        f32x4 e0 = {0, 0, 0, 0}, e1 = e0;
#pragma unroll
        for (int t = 0; t < 8; t += 2) { e0 = mfma16(wo[t], of[t], e0); e1 = mfma16(wo[t + 1], of[t + 1], e1); }
        d = e0 + e1;
        if (!__any(isnan4(d))) break;
        bool miss = false;
#pragma unroll
        for (int t = 0; t < 8; t++) miss |= hassent(of[t]);
        if (!__any(miss)) break;
        if (spin_fail(rw, spins)) break;
      }
      if ((g >> 1) == (n >> 3)) *(f32x4*)(smem + L_O + ((w * 2 + (g >> 1)) * 8 + (n & 7)) * 32 + (g & 1) * 16) = d;
    }
    BAR();
    if (w == 0) {
      const char* b = smem + L_O + (lane >> 5) * 2048 + (lane & 31) * 8;
      f32x2 a = *(const f32x2*)(b);
#pragma unroll
      for (int k = 1; k < 8; k++) a += *(const f32x2*)(b + k * 256);
      auto t0 = __builtin_amdgcn_permlane32_swap(__float_as_uint(a[0]), __float_as_uint(a[0]), false, false);
      auto t1 = __builtin_amdgcn_permlane32_swap(__float_as_uint(a[1]), __float_as_uint(a[1]), false, false);
      const u32 mv = nosent(cvtpk(__uint_as_float(t0[0]) + __uint_as_float(t0[1]), __uint_as_float(t1[0]) + __uint_as_float(t1[1])));
      if (lane < 32) st32(rw, mv, jc * 128 + lane * 4, WS_MB + (s + 1) * 16384);
    }
  }
}

DEVINL u64 uni64(const void* p) {
  const u64 a = (u64)p;
  return ((u64)(u32)__builtin_amdgcn_readfirstlane((int)(u32)(a >> 32)) << 32) | (u32)__builtin_amdgcn_readfirstlane((int)(u32)a);
}
DEVINL void glds16(u64 sbase, u32 voff, u32 ldsaddr) {
  u32 keep;
  asm volatile("s_mov_b32 %0, m0\n\ts_mov_b32 m0, %3\n\ts_nop 0\n\tglobal_load_lds_dwordx4 %1, %2\n\ts_mov_b32 m0, %0"
               : "=&s"(keep) : "v"(voff), "s"(sbase), "s"(ldsaddr) : "memory");
}
template <int N> DEVINL void vmcnt() { asm volatile("s_waitcnt vmcnt(%0)" ::"i"(N) : "memory"); }

template <int BM, int WM, int WN, int TI, int TJ, int NST, bool TRANS>
struct Gemm {
  static constexpr int XB = BM * 128, WB = 64 * 256, STB = XB + WB;
  static constexpr int GX = BM / 64, GW = 2, G = GX + GW;
  f32x16 acc[TI][TJ];
  DEVINL void run(const u16* X, int ldx, const u16* W, int ldw, int nk, char* smem, bool first) {
    const int tid = threadIdx.x, lane = tid & 63;
    const int w = __builtin_amdgcn_readfirstlane(tid >> 6);
    const int wm = w / WN, wn = w % WN;
    const u32 lds0 = __builtin_amdgcn_readfirstlane((u32)(size_t)smem);
    u32 xo[GX], wo[GW];
#pragma unroll
    for (int i = 0; i < GX; i++) {
      const int row = (w * GX + i) * 8 + (lane >> 3);
      xo[i] = (row * ldx + (((lane & 7) ^ ((row >> 1) & 7)) * 8)) * 2;
    }
#pragma unroll
    for (int i = 0; i < GW; i++) {
      const int kr = (w * GW + i) * 4 + (lane >> 4);
      wo[i] = (kr * ldw + (((lane & 15) ^ (((kr & 3) << 2) | ((kr >> 2) & 3))) * 8)) * 2;
    }
    const u64 X64 = uni64(X), W64 = uni64(W);
    const u32 wstep = __builtin_amdgcn_readfirstlane(ldw) * 128;
    auto stage = [&](int kt, int buf) {
      const u64 xb = X64 + (u32)kt * 128;
      const u64 wb = W64 + (u64)((u32)kt * wstep);
#pragma unroll
      for (int i = 0; i < GX; i++) glds16(xb, xo[i], lds0 + buf * STB + (w * GX + i) * 1024);
#pragma unroll
      for (int i = 0; i < GW; i++) glds16(wb, wo[i], lds0 + buf * STB + XB + (w * GW + i) * 1024);
    };
    const int hh = lane >> 5, m = lane & 31;
    const int tm = TRANS ? m : (16 * ((m >> 2) & 1) + 4 * (m >> 3) + (m & 3));
    u32 xa[4];
    {
      const int row = wm * TI * 32 + tm;
#pragma unroll
      for (int s = 0; s < 4; s++) xa[s] = row * 128 + (((2 * s + hh) ^ ((row >> 1) & 7)) * 16);
    }
    u32 wa[TJ][2];
    {
      const int blk = (lane >> 4) & 1, q = (lane & 15) >> 2, pp = lane & 3;
      const int ch = TRANS ? (2 * (pp & 1) + blk) : (2 * blk + (pp >> 1));
      const int half = TRANS ? (pp >> 1) : (pp & 1);
#pragma unroll
      for (int j = 0; j < TJ; j++)
#pragma unroll
        for (int t = 0; t < 2; t++) {
          const int k = 8 * hh + 4 * t + q;
          wa[j][t] = XB + k * 256 + ((((wn * TJ + j) * 4 + ch) ^ ((q << 2) | (2 * hh + t))) * 16) + half * 8;
        }
    }
    if (first) {
#pragma unroll
      for (int i = 0; i < TI; i++)
#pragma unroll
        for (int j = 0; j < TJ; j++)
#pragma unroll
          for (int e = 0; e < 16; e++) acc[i][j][e] = 0.f;
    }
#pragma unroll
    for (int st = 0; st < NST - 1; st++) if (st < nk) stage(st, st);
    int buf = 0;
    for (int kt = 0; kt < nk; kt++) {
      if (kt + NST - 2 < nk) vmcnt<G * (NST - 2)>(); else vmcnt<0>();
      BAR();
      if (kt + NST - 1 < nk) stage(kt + NST - 1, buf == 0 ? NST - 1 : buf - 1);
      const char* sb = smem + buf * STB;
#pragma unroll
      for (int s = 0; s < 4; s++) {
        u32x4 xf[TI], wf[TJ];
#pragma unroll
        for (int i = 0; i < TI; i++) xf[i] = *(const u32x4*)(sb + xa[s] + i * 4096);
#pragma unroll
        for (int j = 0; j < TJ; j++) {
          s16x4 lo = __builtin_amdgcn_ds_read_tr16_b64_v4i16((lds_s16x4)(sb + wa[j][0] + s * 4096));
          s16x4 hi = __builtin_amdgcn_ds_read_tr16_b64_v4i16((lds_s16x4)(sb + wa[j][1] + s * 4096));
          wf[j] = __builtin_bit_cast(u32x4, __builtin_shufflevector(lo, hi, 0, 1, 2, 3, 4, 5, 6, 7));
        }
#pragma unroll
        for (int i = 0; i < TI; i++)
#pragma unroll
          for (int j = 0; j < TJ; j++) acc[i][j] = TRANS ? mfma32(wf[j], xf[i], acc[i][j]) : mfma32(xf[i], wf[j], acc[i][j]);
      }
      buf = buf == NST - 1 ? 0 : buf + 1;
    }
    BAR();
  }
};

DEVINL float mish(float hv) {
  const float u = __expf(-fabsf(hv));
  const float num = hv > 0.f ? 1.f + 2.f * u : u * (u + 2.f);
  const float den = hv > 0.f ? num + 2.f * u * u : num + 2.f;
  return hv * num / den;
}

DEVINL void job_done(rsrc_t rw, int word) {
  VMCNT0();
  BAR();
  if (uni(threadIdx.x >> 6) == 0) __builtin_amdgcn_raw_ptr_buffer_atomic_add_i32(1, rw, lane0off(WS_CT + word * 4), 0, SC1);
}
DEVINL void job_wait(rsrc_t rw, int word, u32 target, int word2, u32 target2) {
  if (uni(threadIdx.x >> 6) == 0) {
    wait_ctr(rw, word, target);
    if (word2 >= 0) wait_ctr(rw, word2, target2);
    __builtin_amdgcn_fence(__ATOMIC_ACQUIRE, "agent");
    VMCNT0();
  }
  BAR();
}

DEVINL void job_proj(const Params& p, rsrc_t rw, char* smem, int kind, int rb, int ct) {
  const int tid = threadIdx.x, lane = tid & 63, w = tid >> 6, wm = w >> 1, wn = w & 1, hh = lane >> 5, m = lane & 31;
  const u16* X = (const u16*)(p.ws + WS_XN) + (size_t)rb * 256 * D_;
  if (kind == 1) {
    Gemm<256, 4, 2, 2, 2, 2, false> gm;
    gm.run(X, D_, p.wv + ct * 128, HD, 16, smem, true);
#pragma unroll
    for (int i = 0; i < 2; i++)
#pragma unroll
      for (int j = 0; j < 2; j++) {
        const int seg = rb * 8 + wm * 2 + i, e = wn * 64 + j * 32 + m;
        const u32 o = (((seg * 32 + ct) * 128 + e) * 32 + 16 * hh) * 2;
        const f32x16 a = gm.acc[i][j];
        st128(rw, u32x4{cvtpk(a[0], a[1]), cvtpk(a[2], a[3]), cvtpk(a[4], a[5]), cvtpk(a[6], a[7])}, o, WS_VT);
        st128(rw, u32x4{cvtpk(a[8], a[9]), cvtpk(a[10], a[11]), cvtpk(a[12], a[13]), cvtpk(a[14], a[15])}, o + 16, WS_VT);
      }
    job_done(rw, CT_KV + rb * 32);
  } else {
    Gemm<256, 4, 2, 2, 2, 2, true> gm;
    const u16* W = kind == 0 ? p.wk : kind == 2 ? p.wq : p.w1;
    const int ldw = kind == 3 ? F_ : HD;
    gm.run(X, D_, W + ct * 128, ldw, 16, smem, true);
    const u32 base = kind == 0 ? WS_KX : kind == 2 ? WS_QX : WS_ACT;
#pragma unroll
    for (int j = 0; j < 2; j++) {
      const int n0 = ct * 128 + wn * 64 + j * 32 + 16 * hh;
      float bias[16];
      if (kind == 3) {
        const u32x4 b0 = *(const u32x4*)(p.b1 + n0), b1 = *(const u32x4*)(p.b1 + n0 + 8);
#pragma unroll
        for (int e = 0; e < 4; e++) { bias[2 * e] = bflo(b0[e]); bias[2 * e + 1] = bfhi(b0[e]); bias[8 + 2 * e] = bflo(b1[e]); bias[9 + 2 * e] = bfhi(b1[e]); }
      }
#pragma unroll
      for (int i = 0; i < 2; i++) {
        const int tok = rb * 256 + wm * 64 + i * 32 + m;
        f32x16 a = gm.acc[i][j];
        if (kind == 3) {
#pragma unroll
          for (int e = 0; e < 16; e++) a[e] = mish(a[e] + bias[e]);
        }
        const u32 o = (tok * ldw + n0) * 2;
        st128(rw, u32x4{cvtpk(a[0], a[1]), cvtpk(a[2], a[3]), cvtpk(a[4], a[5]), cvtpk(a[6], a[7])}, o, base);
        st128(rw, u32x4{cvtpk(a[8], a[9]), cvtpk(a[10], a[11]), cvtpk(a[12], a[13]), cvtpk(a[14], a[15])}, o + 16, base);
      }
    }
    job_done(rw, (kind == 0 ? CT_KV : kind == 2 ? CT_Q : CT_F1) + rb * 32);
  }
}

DEVINL void job_ffn2(const Params& p, rsrc_t rw, char* smem, int rb, int ct) {
  const int tid = threadIdx.x, lane = tid & 63, w = tid >> 6, wm = w >> 1, wn = w & 1, hh = lane >> 5, m = lane & 31;
  job_wait(rw, CT_F1 + rb * 32, 28, -1, 0);
  Gemm<256, 4, 2, 2, 2, 2, true> gm;
  gm.run((const u16*)(p.ws + WS_ACT) + (size_t)rb * 256 * F_, F_, p.w2 + ct * 128, D_, F_ / 64, smem, true);
#pragma unroll
  for (int i = 0; i < 2; i++)
#pragma unroll
    for (int j = 0; j < 2; j++) {
      const int tok = rb * 256 + wm * 64 + i * 32 + m, n0 = ct * 128 + wn * 64 + j * 32 + 16 * hh;
      const u32x4 x0 = *(const u32x4*)(p.x + tok * D_ + n0), x1 = *(const u32x4*)(p.x + tok * D_ + n0 + 8);
      const f32x16 a = gm.acc[i][j];
      const u32 o = (tok * D_ + n0) * 4;
#pragma unroll
      for (int q = 0; q < 4; q++) {
        const u32 xa = q < 2 ? x0[2 * q] : x1[2 * q - 4], xb = q < 2 ? x0[2 * q + 1] : x1[2 * q - 3];
        const f32x4 v = {a[4 * q] + bflo(xa), a[4 * q + 1] + bfhi(xa), a[4 * q + 2] + bflo(xb), a[4 * q + 3] + bfhi(xb)};
        st128(rw, __builtin_bit_cast(u32x4, v), o + q * 16, WS_Y);
      }
    }
  job_done(rw, CT_F2 + rb * 32);
}

DEVINL void job_wo(const Params& p, rsrc_t rw, char* smem, int rb, int ct) {
  const int tid = threadIdx.x, lane = tid & 63, w = tid >> 6, wm = w >> 2, wn = w & 3, hh = lane >> 5, m = lane & 31;
  job_wait(rw, CT_OX + rb * 32, 8, CT_F2 + (rb >> 2) * 32, 8);
  Gemm<64, 2, 4, 1, 1, 4, true> gm;
  gm.run((const u16*)(p.ws + WS_OX) + (size_t)rb * 64 * HD, HD, p.wo + ct * 128, D_, HD / 64, smem, true);
  const int tok = rb * 64 + wm * 32 + m, n0 = ct * 128 + wn * 32 + 16 * hh;
  const f32x4* y = (const f32x4*)(p.ws + WS_Y + (size_t)(tok * D_ + n0) * 4);
  const f32x16 a = gm.acc[0][0];
  u32 o[8];
#pragma unroll
  for (int q = 0; q < 4; q++) {
    const f32x4 yv = y[q];
    o[2 * q] = cvtpk(a[4 * q] + yv[0], a[4 * q + 1] + yv[1]);
    o[2 * q + 1] = cvtpk(a[4 * q + 2] + yv[2], a[4 * q + 3] + yv[3]);
  }
  u32x4* dst = (u32x4*)(p.out + tok * D_ + n0);
  dst[0] = u32x4{o[0], o[1], o[2], o[3]};
  dst[1] = u32x4{o[4], o[5], o[6], o[7]};
}

DEVINL void att_wave(rsrc_t rw, int s, int h, int lane) {
  const int g = lane >> 4, n = lane & 15;
  {
    u32 spins = 0;
    for (;;) {
      CBAR();
      const u32x4 f = ld128(rw, WS_CT + (CT_MF + (s * 32 + h) * 4) * 4, 0);
      if (uni(f[0] & f[1] & f[2] & f[3]) != 0) break;
      if ((++spins & 255) == 0) {
        const u32 t = uni(ld32(rw, WS_CT + CT_TMO * 4, 0));
        if (t != 0 || spins > SPIN_CAP) { st32(rw, 1u, lane0off(WS_CT + CT_TMO * 4), 0); break; }
      }
      __builtin_amdgcn_s_sleep(4);
    }
    CBAR();
  }
  f32x4 sc2[2][3];
#pragma unroll
  for (int a = 0; a < 2; a++)
#pragma unroll
    for (int b = 0; b < 3; b++) sc2[a][b] = f32x4{0, 0, 0, 0};
#pragma unroll
  for (int t = 0; t < 4; t++) {
    const u32 col = (h * DH + 32 * t + 8 * g) * 2;
    const u32x4 q0 = ld128(rw, (32 * s + n) * HD * 2 + col, WS_QX), q1 = ld128(rw, (32 * s + 16 + n) * HD * 2 + col, WS_QX);
    const u32x4 k0 = ld128(rw, (32 * s + n) * HD * 2 + col, WS_KX), k1 = ld128(rw, (32 * s + 16 + n) * HD * 2 + col, WS_KX);
    const u32x4 km = ld128(rw, (((s * 32 + h) * 8 + (n & 7)) * 128 + 32 * t + 8 * g) * 2, WS_KM);
    sc2[0][0] = mfma16(k0, q0, sc2[0][0]); sc2[0][1] = mfma16(k1, q0, sc2[0][1]); sc2[0][2] = mfma16(km, q0, sc2[0][2]);
    sc2[1][0] = mfma16(k0, q1, sc2[1][0]); sc2[1][1] = mfma16(k1, q1, sc2[1][1]); sc2[1][2] = mfma16(km, q1, sc2[1][2]);
  }
  const float sc = 0.08838834764831845f * 1.4426950408889634f;
  u32x4 px[2]; u32x2 pm[2];
#pragma unroll
  for (int a = 0; a < 2; a++) {
    const int qq = 16 * a + n;
    const float ninf = -__builtin_inff();
    float v[12];
#pragma unroll
    for (int i = 0; i < 4; i++) {
      v[i] = (4 * g + i <= qq) ? sc2[a][0][i] : ninf;
      v[4 + i] = (16 + 4 * g + i <= qq) ? sc2[a][1][i] : ninf;
      v[8 + i] = g < 2 ? sc2[a][2][i] : ninf;
    }
    float mx = v[0];
#pragma unroll
    for (int i = 1; i < 12; i++) mx = fmaxf(mx, v[i]);
    mx = xg_max(mx);
    const float mo = mx * sc;
    float sum = 0.f;
#pragma unroll
    for (int i = 0; i < 12; i++) { v[i] = __builtin_amdgcn_exp2f(v[i] * sc - mo); sum += v[i]; }
    sum = xg_sum(sum);
    const float inv = 1.0f / sum;
    px[a] = u32x4{cvtpk(v[0] * inv, v[1] * inv), cvtpk(v[2] * inv, v[3] * inv), cvtpk(v[4] * inv, v[5] * inv), cvtpk(v[6] * inv, v[7] * inv)};
    pm[a] = u32x2{cvtpk(v[8] * inv, v[9] * inv), cvtpk(v[10] * inv, v[11] * inv)};
  }
#pragma unroll
  for (int u = 0; u < 4; u++) {
    f32x4 o[2][2];
#pragma unroll
    for (int T = 0; T < 2; T++) {
      const int e = 32 * u + 8 * (n >> 2) + 4 * T + (n & 3);
      const u32 vo = (((s * 32 + h) * 128 + e) * 32 + 4 * g) * 2;
      const u32x2 lo = ld64(rw, vo, WS_VT), hi = ld64(rw, vo + 32, WS_VT);
      u32x2 vm = ld64(rw, ((s * 32 + h) * 128 + e) * 16 + (g & 1) * 8, WS_VM);
      if (g >= 2) vm = u32x2{0, 0};
      const u32x4 vx = {lo[0], lo[1], hi[0], hi[1]};
      const f32x4 z = {0, 0, 0, 0};
      o[0][T] = mfma16k16(vm, pm[0], mfma16(vx, px[0], z));
      o[1][T] = mfma16k16(vm, pm[1], mfma16(vx, px[1], z));
    }
#pragma unroll
    for (int a = 0; a < 2; a++) {
      const u32x4 ov = {cvtpk(o[a][0][0], o[a][0][1]), cvtpk(o[a][0][2], o[a][0][3]), cvtpk(o[a][1][0], o[a][1][1]), cvtpk(o[a][1][2], o[a][1][3])};
      st128(rw, ov, ((32 * s + 16 * a + n) * HD + h * DH + 32 * u + 8 * g) * 2, WS_OX);
    }
  }
}

constexpr int NJ_KV = 512, NJ_F1 = 224, NJ_Q = 256, NJ_F2 = 64, NJ_TAIL = 32 * 16;
constexpr int J_F1 = NJ_KV, J_Q = J_F1 + NJ_F1, J_F2 = J_Q + NJ_Q, J_TAIL = J_F2 + NJ_F2, J_END = J_TAIL + NJ_TAIL;

DEVINL void worker_role(const Params& p, char* smem) {
  const rsrc_t rw = mkrsrc(p.ws, WS_END);
  int* sjob = (int*)(smem + 147456);
  for (;;) {
    if (uni(threadIdx.x >> 6) == 0) {
      const int t = __builtin_amdgcn_raw_ptr_buffer_atomic_add_i32(1, rw, lane0off(WS_CT + CT_TICKET * 4), 0, SC1);
      *sjob = __builtin_amdgcn_readfirstlane(t);
    }
    BAR();
    const int j = __builtin_amdgcn_readfirstlane(*sjob);
    BAR();
    if (j >= J_END) break;
    if (j < J_F2) {
      int kind, rb, ct;
      if (j < J_F1) { rb = j >> 6; ct = j & 31; kind = (j >> 5) & 1; }
      else if (j < J_Q) { const int i = j - J_F1; rb = i / 28; ct = i % 28; kind = 3; }
      else { const int i = j - J_Q; rb = i >> 5; ct = i & 31; kind = 2; }
      job_proj(p, rw, smem, kind, rb, ct);
    } else if (j < J_TAIL) {
      const int i = j - J_F2;
      job_ffn2(p, rw, smem, i >> 3, i & 7);
    } else {
      const int i = j - J_TAIL, rb = i >> 4, k = i & 15;
      if (k < 8) {
        const int s = rb * 2 + (k >> 2), h = (k & 3) * 8 + (threadIdx.x >> 6);
        job_wait(rw, CT_KV + (s >> 3) * 32, 64, CT_Q + (s >> 3) * 32, 32);
        att_wave(rw, s, h, threadIdx.x & 63);
        job_done(rw, CT_OX + rb * 32);
      } else {
        job_wo(p, rw, smem, rb, k - 8);
      }
    }
  }
}

constexpr int LDS_BYTES = 147456 + 64;

__global__ __launch_bounds__(512, 2) void main_kernel(Params p) {
  extern __shared__ __attribute__((aligned(16))) char smem[];
  if (blockIdx.x < 128) rec_role(p, smem, blockIdx.x);
  else worker_role(p, smem);
}

extern "C" void kernel_launch(void* const* d_in, const int* in_sizes, int n_in, void* d_out, int out_size, void* d_ws,
                              size_t ws_size, hipStream_t stream) {
  Params p;
  p.x = (const u16*)d_in[0];
  p.mem0 = (const u16*)d_in[1];
  p.wq = (const u16*)d_in[2];
  p.wk = (const u16*)d_in[3];
  p.wv = (const u16*)d_in[4];
  p.wo = (const u16*)d_in[5];
  p.w1 = (const u16*)d_in[8];
  p.b1 = (const u16*)d_in[9];
  p.w2 = (const u16*)d_in[10];
  p.out = (u16*)d_out;
  p.ws = (char*)d_ws;
  hipFuncSetAttribute((const void*)main_kernel, hipFuncAttributeMaxDynamicSharedMemorySize, LDS_BYTES);
  prep_kernel<<<512, 256, 0, stream>>>(p);
  main_kernel<<<256, 512, LDS_BYTES, stream>>>(p);
}
```

```cpp
#include <hip/hip_runtime.h>
#include <hip/hip_bf16.h>
#include <stdint.h>

typedef unsigned int u32;
typedef unsigned short u16;
typedef unsigned long long u64;
typedef __attribute__((ext_vector_type(4))) u32 u32x4;
typedef __attribute__((ext_vector_type(2))) u32 u32x2;
typedef __attribute__((ext_vector_type(4))) float f32x4;
typedef __attribute__((ext_vector_type(2))) float f32x2;
typedef __attribute__((ext_vector_type(16))) float f32x16;
typedef __attribute__((ext_vector_type(8))) __bf16 bf16x8;
typedef __attribute__((ext_vector_type(4))) short s16x4;
typedef __attribute__((ext_vector_type(8))) short s16x8;
typedef __attribute__((address_space(3))) s16x4* lds_s16x4;

typedef __attribute__((address_space(3))) volatile int* lds_vint;
typedef __attribute__((address_space(3))) volatile u32x4* lds_vu32x4;

#define DEVINL __device__ __forceinline__
constexpr int S_ = 2048, D_ = 1024, H_ = 32, DH = 128, HD = 4096, F_ = 3584, NSEG = 64;
constexpr u32 WS_XN = 0;
constexpr u32 WS_QX = WS_XN + (4u << 20);
constexpr u32 WS_KX = WS_QX + (16u << 20);
constexpr u32 WS_VT = WS_KX + (16u << 20);
constexpr u32 WS_ACT = WS_VT + (16u << 20);
constexpr u32 WS_Y = WS_ACT + (14u << 20);
constexpr u32 WS_OX = WS_Y + (8u << 20);
constexpr u32 WS_KM = WS_OX + (16u << 20);
constexpr u32 WS_VM = WS_KM + (4u << 20);
constexpr u32 WS_CT = WS_VM + (4u << 20);
constexpr u32 CT_BYTES = 64u << 10;
constexpr u32 WS_SL = WS_CT + CT_BYTES;
constexpr int NSK = 2;
constexpr u32 WS_X = WS_SL + NSK * 64 * 32768;
#define RING 64
constexpr int RM = RING - 1;
#define LAZY 6
constexpr u32 X_MB = 0;
constexpr u32 X_OB = X_MB + RING * 2 * 16384;
constexpr u32 X_SB = X_OB + RING * 8 * 16384;
constexpr u32 X_BYTES = X_SB + RING * 32 * 16384;
constexpr u32 WS_END = WS_X + X_BYTES + 16384;
DEVINL u32 xbase(const char* ws) { return WS_X + ((16384u - (u32)(((u64)ws + WS_X) & 16383u)) & 16383u); }
constexpr int CT_TICKET = 0, CT_TMO = 32, CT_KV = 64, CT_Q = CT_KV + 8 * 32, CT_F1 = CT_Q + 8 * 32, CT_F2 = CT_F1 + 8 * 32,
              CT_OX = CT_F2 + 8 * 32, CT_KV0 = CT_OX + 32 * 32, CT_PREP = CT_KV0 + 4 * 32, CT_SK = CT_PREP + 32, CT_RA = CT_SK + NSK * 8 * 32, CT_RB = CT_RA + 32, CT_PAR = CT_RB + 32,
              CT_CI = CT_PAR + 32,
              CT_MF = 4096;
constexpr u32 SENT = 0xFFFFFFFFu;
constexpr u32 SPIN_CAP = 1u << 18;

struct Params {
  const u16 *x, *mem0, *wq, *wk, *wv, *wo, *w1, *b1, *w2;
  u16* out;
  char* ws;
};

DEVINL float bf2f(u16 v) { return __uint_as_float((u32)v << 16); }
DEVINL float bflo(u32 v) { return __uint_as_float(v << 16); }
DEVINL float bfhi(u32 v) { return __uint_as_float(v & 0xFFFF0000u); }
DEVINL u32 cvtpk(float lo, float hi) {
  typedef __attribute__((ext_vector_type(2))) __bf16 bf16x2;
  bf16x2 r = {(__bf16)lo, (__bf16)hi};
  return __builtin_bit_cast(u32, r);
}
DEVINL float rbf(float v) { return bflo(cvtpk(v, v)); }
DEVINL u32 nosent(u32 v) { return v < 0xFFFFFFFEu ? v : 0xFFFFFFFEu; }
DEVINL f32x4 mfma16(u32x4 a, u32x4 b, f32x4 c) {
  return __builtin_amdgcn_mfma_f32_16x16x32_bf16(__builtin_bit_cast(bf16x8, a), __builtin_bit_cast(bf16x8, b), c, 0, 0, 0);
}
DEVINL f32x4 mfma16k16(u32x2 a, u32x2 b, f32x4 c) {
  return mfma16(u32x4{a[0], a[1], 0, 0}, u32x4{b[0], b[1], 0, 0}, c);
}
DEVINL f32x16 mfma32(u32x4 a, u32x4 b, f32x16 c) {
  return __builtin_amdgcn_mfma_f32_32x32x16_bf16(__builtin_bit_cast(bf16x8, a), __builtin_bit_cast(bf16x8, b), c, 0, 0, 0);
}
DEVINL float max3(float a, float b, float c) { float r; asm("v_max3_f32 %0, %1, %2, %3" : "=v"(r) : "v"(a), "v"(b), "v"(c)); return r; }
DEVINL float max2(float a, float b) { float r; asm("v_max_f32 %0, %1, %2" : "=v"(r) : "v"(a), "v"(b)); return r; }
DEVINL float xg_max(float x) {
  auto s = __builtin_amdgcn_permlane16_swap(__float_as_uint(x), __float_as_uint(x), false, false);
  x = max2(__uint_as_float(s[0]), __uint_as_float(s[1]));
  auto t = __builtin_amdgcn_permlane32_swap(__float_as_uint(x), __float_as_uint(x), false, false);
  return max2(__uint_as_float(t[0]), __uint_as_float(t[1]));
}
DEVINL float xg_sum(float x) {
  auto s = __builtin_amdgcn_permlane16_swap(__float_as_uint(x), __float_as_uint(x), false, false);
  x = __uint_as_float(s[0]) + __uint_as_float(s[1]);
  auto t = __builtin_amdgcn_permlane32_swap(__float_as_uint(x), __float_as_uint(x), false, false);
  return __uint_as_float(t[0]) + __uint_as_float(t[1]);
}
typedef __amdgpu_buffer_rsrc_t rsrc_t;
DEVINL rsrc_t mkrsrc(const void* p, u32 n) { return __builtin_amdgcn_make_buffer_rsrc((void*)p, 0, n, 0x00020000); }
constexpr int SC1 = 16;
DEVINL u32x4 ld128(rsrc_t r, u32 vo, u32 so) { return __builtin_amdgcn_raw_buffer_load_b128(r, vo, so, SC1); }
DEVINL u32x2 ld64(rsrc_t r, u32 vo, u32 so) { return __builtin_amdgcn_raw_buffer_load_b64(r, vo, so, SC1); }
DEVINL u32 ld32(rsrc_t r, u32 vo, u32 so) { return __builtin_amdgcn_raw_buffer_load_b32(r, vo, so, SC1); }
DEVINL void st128(rsrc_t r, u32x4 v, u32 vo, u32 so) { __builtin_amdgcn_raw_buffer_store_b128(v, r, vo + so, 0, SC1); }
DEVINL void st64(rsrc_t r, u32x2 v, u32 vo, u32 so) { __builtin_amdgcn_raw_buffer_store_b64(v, r, vo, so, SC1); }
DEVINL void st32(rsrc_t r, u32 v, u32 vo, u32 so) { __builtin_amdgcn_raw_buffer_store_b32(v, r, vo, so, SC1); }
#define CBAR() asm volatile("" ::: "memory")
#define VMCNT0() asm volatile("s_waitcnt vmcnt(0)" ::: "memory")
#define BAR() do { asm volatile("s_waitcnt lgkmcnt(0)" ::: "memory"); __builtin_amdgcn_s_barrier(); CBAR(); } while (0)
DEVINL bool isnan4(f32x4 v) { float s = (v[0] + v[1]) + (v[2] + v[3]); return s != s; }
DEVINL u32 umax(u32 a, u32 b) { return a > b ? a : b; }
DEVINL u32 max4(u32x4 v) { return umax(umax(v[0], v[1]), umax(v[2], v[3])); }
#define SCHED0() __builtin_amdgcn_sched_barrier(0)
DEVINL void nap(int n) { for (int i = 0; i < n; i++) __builtin_amdgcn_s_sleep(1); }

constexpr u32 OOB = 0x7FFFFFF0u;
DEVINL u32 lane0off(u32 off) { return (threadIdx.x & 63) == 0 ? off : OOB; }
DEVINL u32 uni(u32 v) { return (u32)__builtin_amdgcn_readfirstlane((int)v); }
DEVINL void wait_ctr(rsrc_t rw, int word, u32 target) {
  u32 spins = 0;
  for (;;) {
    CBAR();
    if (uni(ld32(rw, WS_CT + word * 4, 0)) >= target) break;
    if ((++spins & 255) == 0) {
      const u32 t = uni(ld32(rw, WS_CT + CT_TMO * 4, 0));
      if (t != 0 || spins > SPIN_CAP) { st32(rw, 1u, lane0off(WS_CT + CT_TMO * 4), 0); break; }
    }
    __builtin_amdgcn_s_sleep(4);
  }
}

DEVINL bool spin_fail(rsrc_t rw, u32& spins) {
  if ((++spins & 63) != 0) return false;
  const u32 t = ld32(rw, WS_CT + CT_TMO * 4, 0);
  if (uni(t) == 0 && spins < SPIN_CAP) return false;
  st32(rw, 1u, lane0off(WS_CT + CT_TMO * 4), 0);
  return true;
}

#define STAMP(i)
#define SPINS(i)
#define WSTAMP(idx)

DEVINL void prep_role(const Params& p, rsrc_t rw, int wb) {
  const int tid = threadIdx.x, lane = tid & 63, w = tid >> 6;
#pragma unroll
  for (int rr = 0; rr < 2; rr++) {
    const int row = wb * 16 + w * 2 + rr;
    const u32x4* xr = (const u32x4*)(p.x + (size_t)row * D_);
    u32x4 a = xr[lane], b = xr[64 + lane];
    float v[16];
#pragma unroll
    for (int i = 0; i < 4; i++) { v[2 * i] = bflo(a[i]); v[2 * i + 1] = bfhi(a[i]); v[8 + 2 * i] = bflo(b[i]); v[9 + 2 * i] = bfhi(b[i]); }
    float s = 0.f;
#pragma unroll
    for (int i = 0; i < 16; i++) s += v[i];
#pragma unroll
    for (int o = 32; o > 0; o >>= 1) s += __shfl_xor(s, o);
    const float mean = s * (1.0f / D_);
    float q = 0.f;
#pragma unroll
    for (int i = 0; i < 16; i++) { float d = v[i] - mean; q += d * d; }
#pragma unroll
    for (int o = 32; o > 0; o >>= 1) q += __shfl_xor(q, o);
    const float mb = rbf(mean), den = rbf(sqrtf(rbf(rbf(q * (1.0f / D_)) + 1e-5f)));
    u32x4 oa, ob;
#pragma unroll
    for (int i = 0; i < 4; i++) {
      oa[i] = cvtpk(rbf(v[2 * i] - mb) / den, rbf(v[2 * i + 1] - mb) / den);
      ob[i] = cvtpk(rbf(v[8 + 2 * i] - mb) / den, rbf(v[9 + 2 * i] - mb) / den);
    }
    st128(rw, oa, row * 2048 + lane * 16, WS_XN);
    st128(rw, ob, row * 2048 + 1024 + lane * 16, WS_XN);
  }
  const u32 xb = xbase(p.ws);
  const u32x4 sv = {SENT, SENT, SENT, SENT};
  constexpr u32 NS = LAZY ? LAZY + 1 : RING;
  for (u32 i = wb * 512 + tid; i < (NS - 1) * 2 * 1024; i += 128 * 512) st128(rw, sv, i * 16, xb + X_MB + 2 * 16384);
  for (u32 i = wb * 512 + tid; i < NS * 8 * 1024; i += 128 * 512) st128(rw, sv, i * 16, xb + X_OB);
  for (u32 i = wb * 512 + tid; i < NS * 32 * 640; i += 128 * 512) {
    const u32 pr = i / 640, k = i - pr * 640;
    st128(rw, sv, pr * 16384 + (k >= 320 ? 8192 + (k - 320) * 16 : k * 16), xb + X_SB);
  }
  if (wb < 2) {
    const int i = wb * 512 + tid;
    const u32x4 v = *(const u32x4*)(p.mem0 + (i & 7) * D_ + (i >> 3) * 8);
    st128(rw, v, tid * 16, xb + X_MB + wb * 16384);
    st128(rw, v, tid * 16 + 8192, xb + X_MB + wb * 16384);
  }
}

constexpr int L_QK = 0, L_V = 8192, L_O = 16384, L_KV = 20480, L_VM = 28672, L_VF = 29696;
#define D_E1 10
#define D_E2 1
#define D_E3 6

DEVINL void rec_prefetch(rsrc_t rw, char* smem, int s, int h, int c, int lane) {
  const int g = lane >> 4, m = lane & 15;
  if (s < 8) { if ((s & 1) == 0) { wait_ctr(rw, CT_KV0 + (s >> 1) * 32, 64); CBAR(); } }
  else if ((s & 7) == 0) { wait_ctr(rw, CT_KV + (s >> 3) * 32, 64); CBAR(); }
  u32x4 k0 = ld128(rw, ((32 * s + m) * HD + h * DH + c * 32 + 8 * g) * 2, WS_KX);
  u32x4 k1 = ld128(rw, ((32 * s + 16 + m) * HD + h * DH + c * 32 + 8 * g) * 2, WS_KX);
  u32x4 v[2];
#pragma unroll
  for (int T = 0; T < 2; T++) {
    u32 o = (((s * 32 + h) * 128 + c * 32 + 16 * T + m) * 32 + 4 * g) * 2;
    u32x2 lo = ld64(rw, o, WS_VT), hi = ld64(rw, o + 32, WS_VT);
    v[T] = u32x4{lo[0], lo[1], hi[0], hi[1]};
  }
  u32x4* dst = (u32x4*)(smem + L_KV + (s & 1) * 4096);
  dst[lane] = k0; dst[64 + lane] = k1; dst[128 + lane] = v[0]; dst[192 + lane] = v[1];
}

DEVINL void rec_role(const Params& p, char* smem, int r) {
  const int tid = threadIdx.x, lane0 = tid & 63;
  const int w = __builtin_amdgcn_readfirstlane(tid >> 6);
  const int h = (r & 7) + 8 * (r >> 5), c = (r >> 3) & 3, jc = (r & 7) * 16 + (r >> 3);
  const int nh = w >> 2, kq = w & 3;
  const rsrc_t rw = mkrsrc(p.ws, WS_END);
  const u32 xb = xbase(p.ws);
  const u32 pg0 = (u32)(((u64)p.ws + xb) >> 21);
  const int npg = (int)((u32)(((u64)p.ws + xb + X_BYTES - 1) >> 21) - pg0) + 1;
  u32 xcc;
  asm volatile("s_getreg_b32 %0, hwreg(HW_REG_XCC_ID, 0, 4)" : "=s"(xcc));
  const u32 iod = (xcc >> 2) & 1;
  if (r < npg) {
    u32 o = (u32)((((u64)(pg0 + r)) << 21) - (u64)p.ws);
    o = (o < xb || o > 0x80000000u) ? xb : o;
    o = xb + ((o - xb + 16383u) & ~16383u);
    if (o + 16384 > xb + X_BYTES) o -= 16384;
    u32 d[2];
#pragma unroll
    for (int k = 0; k < 2; k++) {
      const int hf = (k ^ w) & 1;
      const char* a = p.ws + o + 7168 + hf * 8192 + w * 128;
      u32 v;
      const u64 t0 = __builtin_amdgcn_s_memtime();
      asm volatile("s_waitcnt lgkmcnt(0)\n\tglobal_load_dword %0, %1, off sc1\n\ts_waitcnt vmcnt(0)" : "=v"(v) : "v"(a) : "memory");
      const u64 t1 = __builtin_amdgcn_s_memtime();
      d[k] = (u32)(t1 - t0);
    }
    *(lds_vint)(smem + L_V + w * 8 + (w & 1) * 4) = (int)d[0];
    *(lds_vint)(smem + L_V + w * 8 + ((w & 1) ^ 1) * 4) = (int)d[1];
    BAR();
    if (w == 0) {
      u32 b0 = ~0u, b1 = ~0u;
      for (int k = 0; k < 8; k++) {
        const u32 x = (u32)*(lds_vint)(smem + L_V + k * 8), y = (u32)*(lds_vint)(smem + L_V + k * 8 + 4);
        b0 = x < b0 ? x : b0; b1 = y < b1 ? y : b1;
      }
      const u32 bit = (uni(b0) <= uni(b1) ? 0u : 1u) ^ iod;
      __builtin_amdgcn_raw_ptr_buffer_atomic_add_i32((int)(bit << r), rw, lane0off(WS_CT + CT_PAR * 4), 0, SC1);
    }
  }
  if (w == 0 && c == 0) st32(rw, 1u + iod, lane0off(WS_CT + (CT_CI + h) * 4), 0);
  u32x4 wqk[2][8], wv[8], wo[8];
  {
    const int lane = lane0, g = lane >> 4, q = (lane & 15) >> 2, pp = lane & 3;
    const u16* W = (nh ? p.wk : p.wq) + (size_t)(256 * kq) * HD + h * DH + c * 32;
    const u16* V = p.wv + (size_t)(256 * kq) * HD + h * DH + c * 32 + 16 * nh;
    const u16* O = p.wo + (size_t)(512 * w) * D_ + 8 * jc;
    u32x4 ra[16], rb[8], rc[8];
#pragma unroll
    for (int i = 0; i < 16; i++) ra[i] = *(const u32x4*)(W + (size_t)(16 * i + (lane >> 2)) * HD + (lane & 3) * 8);
#pragma unroll
    for (int i = 0; i < 8; i++) rb[i] = *(const u32x4*)(V + (size_t)(32 * i + (lane >> 1)) * HD + (lane & 1) * 8);
#pragma unroll
    for (int i = 0; i < 8; i++) rc[i] = *(const u32x4*)(O + (size_t)(64 * i + lane) * D_);
    SCHED0();
    SCHED0();
    char* ar = smem + w * 16384;
    BAR();
#pragma unroll
    for (int i = 0; i < 16; i++) *(u32x4*)(ar + i * 1024 + lane * 16) = ra[i];
    asm volatile("s_waitcnt lgkmcnt(0)" ::: "memory");
    auto tr = [&](const char* a0, const char* a1) -> u32x4 {
      const s16x4 lo = __builtin_amdgcn_ds_read_tr16_b64_v4i16((lds_s16x4)a0), hi = __builtin_amdgcn_ds_read_tr16_b64_v4i16((lds_s16x4)a1);
      return __builtin_bit_cast(u32x4, __builtin_shufflevector(lo, hi, 0, 1, 2, 3, 4, 5, 6, 7));
    };
#pragma unroll
    for (int t = 0; t < 8; t++)
#pragma unroll
      for (int T = 0; T < 2; T++) {
        const char* a = ar + (32 * t + 8 * g + q) * 64 + (16 * T + 4 * pp) * 2;
        wqk[T][t] = tr(a, a + 4 * 64);
      }
    asm volatile("s_waitcnt lgkmcnt(0)" ::: "memory");
    SCHED0();
#pragma unroll
    for (int i = 0; i < 8; i++) { *(u32x4*)(ar + i * 1024 + lane * 16) = rb[i]; *(u32x4*)(ar + 8192 + i * 1024 + lane * 16) = rc[i]; }
    asm volatile("s_waitcnt lgkmcnt(0)" ::: "memory");
#pragma unroll
    for (int t = 0; t < 8; t++) {
      const char* a = ar + (32 * t + 8 * g + q) * 32 + pp * 8;
      wv[t] = tr(a, a + 4 * 32);
      const char* b = ar + 8192 + (256 * (pp >> 1) + 32 * t + 8 * g + q) * 16 + (pp & 1) * 8;
      wo[t] = tr(b, b + 4 * 16);
    }
    asm volatile("s_waitcnt lgkmcnt(0)" ::: "memory");
  }
  VMCNT0();
  BAR();
  if (w == 0) {
    __builtin_amdgcn_raw_ptr_buffer_atomic_add_i32(1, rw, lane0off(WS_CT + CT_RA * 4), 0, SC1);
    wait_ctr(rw, CT_RA, 128);
    wait_ctr(rw, CT_PREP, 128);
    CBAR();
    const u32 pm = uni(ld32(rw, WS_CT + CT_PAR * 4, 0)), ci = uni(ld32(rw, WS_CT + (CT_CI + h) * 4, 0));
    *(lds_vint)(smem + L_VF + 16) = (int)pm;
    *(lds_vint)(smem + L_VF + 20) = (int)(ci - 1);
  }
  if (w == 7) rec_prefetch(rw, smem, 0, h, c, lane0);
  if (w == 1) *(lds_vint)(smem + L_VF) = 0;
  BAR();
  const u32 parmask = uni(*(lds_vint)(smem + L_VF + 16)), ciod = uni(*(lds_vint)(smem + L_VF + 20)) & 1;
  auto nearhalf = [&](u32 o, u32 d) -> u32 {
    const u32 pg = (u32)(((u64)p.ws + o) >> 21) - pg0;
    return o + ((((parmask >> pg) & 1u) ^ d) << 13);
  };
  const float scale = 0.08838834764831845f;
  const float l2e = 1.4426950408889634f;

  for (int s = 0; s < NSEG; s++) {
    int lane = lane0;
    asm volatile("" : "+v"(lane));
    const int g = lane >> 4, n = lane & 15;
    STAMP(0);
    u32x4 mf[8];
    f32x4 d0, d1;
    {
      const u32 vo = ((kq & 1) * 32 + g) * 128 + (n & 7) * 16, so = nearhalf(xb + X_MB + ((s & RM) * 2 + (kq >> 1)) * 16384, iod);
      u32 spins = 0;
      if (s > 0) nap(D_E1);
      for (;;) {
        CBAR();
#pragma unroll
        for (int t = 0; t < 8; t++) mf[t] = ld128(rw, vo, so + t * 512);
        SCHED0();
        d0 = f32x4{0, 0, 0, 0}; d1 = d0;
#pragma unroll
        for (int t = 0; t < 8; t++) { d0 = mfma16(wqk[0][t], mf[t], d0); d1 = mfma16(wqk[1][t], mf[t], d1); }
        if (!__any(isnan4(d0 + d1))) break;
        u32 mxw = 0;
#pragma unroll
        for (int t = 0; t < 8; t++) mxw = umax(mxw, max4(mf[t]));
        if (!__any(mxw == SENT)) break;
        if (spin_fail(rw, spins)) break;
      }
      SPINS(10);
    }
    STAMP(1);
    {
      f32x4 dv = n < 8 ? d0 : d1;
      *(f32x4*)(smem + L_QK + kq * 2048 + nh * 1024 + (n >> 3) * 512 + g * 128 + (n & 7) * 16) = dv;
    }
    BAR();
    STAMP(2);
    u32x4 F;
    if (w == 0) {
      const char* b = smem + L_QK + (n >> 3) * 1024 + (g >> 1) * 512 + (g & 1) * 256 + (n & 7) * 16;
      f32x4 a0 = *(const f32x4*)(b), a1 = *(const f32x4*)(b + 128);
#pragma unroll
      for (int k = 1; k < 4; k++) { a0 += *(const f32x4*)(b + k * 2048); a1 += *(const f32x4*)(b + k * 2048 + 128); }
      F = u32x4{cvtpk(a0[0], a0[1]), cvtpk(a0[2], a0[3]), cvtpk(a1[0], a1[1]), cvtpk(a1[2], a1[3])};
      if (s < NSEG - 1) {
        const u32x4* kv = (const u32x4*)(smem + L_KV + (s & 1) * 4096);
        const f32x4 z = {0, 0, 0, 0};
        f32x4 sx0 = mfma16(kv[lane], F, z), sx1 = mfma16(kv[64 + lane], F, z), sm = mfma16(F, F, z);
        const u32 so = nearhalf(xb + X_SB + ((s & RM) * 32 + h) * 16384, ciod) + c * 1280;
        u32x4 u0, u1, u2;
#pragma unroll
        for (int i = 0; i < 4; i++) {
          u0[i] = nosent(__float_as_uint(sx0[i])); u1[i] = nosent(__float_as_uint(sx1[i])); u2[i] = nosent(__float_as_uint(sm[i]));
        }
        if (n < 8) {
          st128(rw, u0, n * 64 + g * 16, so);
          st128(rw, u1, 512 + n * 64 + g * 16, so);
          if (g >= 2) st128(rw, u2, 1024 + n * 32 + (g - 2) * 16, so);
        }
      }
    }
    STAMP(3);
    {
      f32x4 dv = {0, 0, 0, 0};
#pragma unroll
      for (int t = 0; t < 8; t++) dv = mfma16(mf[t], wv[t], dv);
      *(f32x4*)(smem + L_V + kq * 2048 + nh * 1024 + lane * 16) = dv;
    }
    BAR();
    if (w == 1) {
      u32x2 vm[2];
#pragma unroll
      for (int T = 0; T < 2; T++) {
        const char* b = smem + L_V + T * 1024 + lane * 16;
        f32x4 a = *(const f32x4*)(b);
#pragma unroll
        for (int k = 1; k < 4; k++) a += *(const f32x4*)(b + k * 2048);
        vm[T] = u32x2{cvtpk(a[0], a[1]), cvtpk(a[2], a[3])};
      }
      *(u32x4*)(smem + L_VM + lane * 16) = u32x4{vm[0][0], vm[0][1], vm[1][0], vm[1][1]};
      asm volatile("s_waitcnt lgkmcnt(0)" ::: "memory");
      *(lds_vint)(smem + L_VF) = s + 1;
      const char* b = smem + L_QK + 1024 + (g >> 1) * 512 + (g & 1) * 256 + (n & 7) * 16;
      f32x4 a0 = *(const f32x4*)(b), a1 = *(const f32x4*)(b + 128);
#pragma unroll
      for (int k = 1; k < 4; k++) { a0 += *(const f32x4*)(b + k * 2048); a1 += *(const f32x4*)(b + k * 2048 + 128); }
      const u32x4 km = {cvtpk(a0[0], a0[1]), cvtpk(a0[2], a0[3]), cvtpk(a1[0], a1[1]), cvtpk(a1[2], a1[3])};
      if (n < 8) st128(rw, km, (((s * 32 + h) * 8 + n) * 128 + c * 32 + 8 * g) * 2, WS_KM);
#pragma unroll
      for (int T = 0; T < 2; T++) {
        auto s0 = __builtin_amdgcn_permlane16_swap(vm[T][0], vm[T][0], false, false);
        auto s1 = __builtin_amdgcn_permlane16_swap(vm[T][1], vm[T][1], false, false);
        if (g == 2) st128(rw, u32x4{s0[0], s1[0], s0[1], s1[1]}, ((s * 32 + h) * 128 + c * 32 + 16 * T + n) * 16, WS_VM);
      }
      if (RING < NSEG || (LAZY > 0 && s + LAZY + 1 < NSEG)) {
        const int sa = LAZY ? s + LAZY : (s - 2) & RM, sm_ = LAZY ? s + LAZY + 1 : (s - 1) & RM;
        const u32x4 sv = {SENT, SENT, SENT, SENT};
        const u32 ss = nearhalf(xb + X_SB + (sa * 32 + h) * 16384, ciod) + c * 1280;
        st128(rw, sv, lane * 16, ss);
        st128(rw, sv, lane < 16 ? 1024 + lane * 16 : OOB - ss, ss);
        const int oct = 2 * (n >> 3) + (g >> 1);
        const u32 line = (((h >> 2) * 8 + (h & 1) * 4 + c) * 4 + oct) * 256 + ((h >> 1) & 1) * 128;
        st128(rw, sv, (line & 8191) + (n & 7) * 16 + (g & 1) * 8192, xb + X_OB + (sa * 8 + (line >> 13)) * 16384);
        const u32 ms = xb + X_MB + (sm_ * 2 + (jc >> 6)) * 16384;
        st128(rw, sv, lane < 16 ? (jc & 63) * 128 + (lane & 7) * 16 + (lane >> 3) * 8192 : OOB - ms, ms);
      }
      VMCNT0();
      st32(rw, 1u, lane0off(WS_CT + (CT_MF + (s * 32 + h) * 4 + c) * 4), 0);
    }
    if (w == 0) {
      if (s < NSEG - 1) {
        const u32x4* kv = (const u32x4*)(smem + L_KV + (s & 1) * 4096);
        f32x4 sx0, sx1, sm;
        const u32 sb = nearhalf(xb + X_SB + ((s & RM) * 32 + h) * 16384, ciod);
        const u32 vo = (n & 7) * 64 + g * 16, vm_o = 1024 + (n & 7) * 32 + (g & 1) * 16;
        u32 spins = 0;
        STAMP(4);
        const u32x4 vx0 = kv[128 + lane], vx1 = kv[192 + lane];
        nap(D_E2);
        int vflag = *(lds_vint)(smem + L_VF);
        u32x4 vmm = *(lds_vu32x4)(smem + L_VM + lane * 16);
        if (g < 2) vmm = u32x4{0, 0, 0, 0};
        for (;;) {
          CBAR();
          u32x4 a[4], b[4], e[4];
#pragma unroll
          for (int k = 0; k < 4; k++) {
            const u32 so = sb + k * 1280;
            a[k] = ld128(rw, vo, so); b[k] = ld128(rw, vo, so + 512); e[k] = ld128(rw, vm_o, so);
          }
          SCHED0();
          sx0 = (__builtin_bit_cast(f32x4, a[0]) + __builtin_bit_cast(f32x4, a[1])) + (__builtin_bit_cast(f32x4, a[2]) + __builtin_bit_cast(f32x4, a[3]));
          sx1 = (__builtin_bit_cast(f32x4, b[0]) + __builtin_bit_cast(f32x4, b[1])) + (__builtin_bit_cast(f32x4, b[2]) + __builtin_bit_cast(f32x4, b[3]));
          sm = (__builtin_bit_cast(f32x4, e[0]) + __builtin_bit_cast(f32x4, e[1])) + (__builtin_bit_cast(f32x4, e[2]) + __builtin_bit_cast(f32x4, e[3]));
          if (!__any(isnan4(sx0 + sx1 + sm))) break;
          u32 mxw = 0;
#pragma unroll
          for (int k = 0; k < 4; k++) mxw = umax(mxw, umax(max4(a[k]), umax(max4(b[k]), max4(e[k]))));
          if (!__any(mxw == SENT)) break;
          if (spin_fail(rw, spins)) break;
        }
        SPINS(11);
        STAMP(5);
        const float sc = scale * l2e;
        float mx = max3(max3(sx0[0], sx0[1], sx0[2]), max3(sx0[3], sx1[0], sx1[1]), max3(sx1[2], sx1[3], sm[0]));
        mx = max3(mx, max3(sm[1], sm[2], sm[3]), mx);
        mx = xg_max(mx);
        const float mo = mx * sc;
#pragma unroll
        for (int i = 0; i < 4; i++) {
          sx0[i] = __builtin_amdgcn_exp2f(sx0[i] * sc - mo);
          sx1[i] = __builtin_amdgcn_exp2f(sx1[i] * sc - mo);
          sm[i] = __builtin_amdgcn_exp2f(sm[i] * sc - mo);
        }
        const u32x4 px = {cvtpk(sx0[0], sx0[1]), cvtpk(sx0[2], sx0[3]), cvtpk(sx1[0], sx1[1]), cvtpk(sx1[2], sx1[3])};
        u32x2 pm = {cvtpk(sm[0], sm[1]), cvtpk(sm[2], sm[3])};
        if (g < 2) pm = u32x2{0, 0};
        while (__builtin_amdgcn_readfirstlane(vflag) != s + 1) {
          vflag = *(lds_vint)(smem + L_VF);
          vmm = *(lds_vu32x4)(smem + L_VM + lane * 16);
          if (g < 2) vmm = u32x4{0, 0, 0, 0};
        }
        const f32x4 z = {0, 0, 0, 0};
        f32x4 o0 = mfma16k16(u32x2{vmm[0], vmm[1]}, pm, mfma16(vx0, px, z));
        f32x4 o1 = mfma16k16(u32x2{vmm[2], vmm[3]}, pm, mfma16(vx1, px, z));
        float sum = ((bflo(px[0]) + bfhi(px[0])) + (bflo(px[1]) + bfhi(px[1]))) + ((bflo(px[2]) + bfhi(px[2])) + (bflo(px[3]) + bfhi(px[3])));
        sum += (bflo(pm[0]) + bfhi(pm[0])) + (bflo(pm[1]) + bfhi(pm[1]));
        const float inv = __builtin_amdgcn_rcpf(xg_sum(sum));
        f32x4 oo = n < 8 ? o0 : o1;
        const u32x2 ov = {nosent(cvtpk(oo[0] * inv, oo[1] * inv)), nosent(cvtpk(oo[2] * inv, oo[3] * inv))};
        const int oct = 2 * (n >> 3) + (g >> 1);
        const u32 line = (((h >> 2) * 8 + (h & 1) * 4 + c) * 4 + oct) * 256 + ((h >> 1) & 1) * 128;
        auto s0 = __builtin_amdgcn_permlane16_swap(ov[0], ov[0], false, false);
        auto s1 = __builtin_amdgcn_permlane16_swap(ov[1], ov[1], false, false);
        st128(rw, u32x4{s0[0], s1[0], s0[1], s1[1]}, (line & 8191) + (n & 7) * 16 + (g & 1) * 8192, xb + X_OB + ((s & RM) * 8 + (line >> 13)) * 16384);
      }
    } else if (w == 7) {
      if (s + 1 < NSEG - 1) rec_prefetch(rw, smem, s + 1, h, c, lane);
    }
    STAMP(6);
    if (s == NSEG - 1) break;
    BAR();
    {
      f32x4 d;
      const u32 vo = lane * 16, so = nearhalf(xb + X_OB + ((s & RM) * 8 + w) * 16384, iod);
      u32 spins = 0;
      nap(D_E3);
      for (;;) {
        CBAR();
        u32x4 of[8];
#pragma unroll
        for (int t = 0; t < 8; t++) of[t] = ld128(rw, vo, so + t * 1024);
        SCHED0();
        f32x4 e0 = {0, 0, 0, 0}, e1 = e0;
#pragma unroll
        for (int t = 0; t < 8; t += 2) { e0 = mfma16(wo[t], of[t], e0); e1 = mfma16(wo[t + 1], of[t + 1], e1); }
        d = e0 + e1;
        if (!__any(isnan4(d))) break;
        u32 mxw = 0;
#pragma unroll
        for (int t = 0; t < 8; t++) mxw = umax(mxw, max4(of[t]));
        if (!__any(mxw == SENT)) break;
        if (spin_fail(rw, spins)) break;
      }
      SPINS(12);
      STAMP(7);
      if ((g >> 1) == (n >> 3)) *(f32x4*)(smem + L_O + ((w * 2 + (g >> 1)) * 8 + (n & 7)) * 32 + (g & 1) * 16) = d;
    }
    BAR();
    STAMP(8);
    if (w == 0) {
      const char* b = smem + L_O + (lane >> 5) * 2048 + (lane & 31) * 8;
      f32x2 a = *(const f32x2*)(b);
#pragma unroll
      for (int k = 1; k < 8; k++) a += *(const f32x2*)(b + k * 256);
      auto t0 = __builtin_amdgcn_permlane32_swap(__float_as_uint(a[0]), __float_as_uint(a[0]), false, false);
      auto t1 = __builtin_amdgcn_permlane32_swap(__float_as_uint(a[1]), __float_as_uint(a[1]), false, false);
      const u32 mv = nosent(cvtpk(__uint_as_float(t0[0]) + __uint_as_float(t0[1]), __uint_as_float(t1[0]) + __uint_as_float(t1[1])));
      const u32x4 m4 = {(u32)__builtin_amdgcn_mov_dpp((int)mv, 0x00, 0xf, 0xf, true), (u32)__builtin_amdgcn_mov_dpp((int)mv, 0x55, 0xf, 0xf, true),
                        (u32)__builtin_amdgcn_mov_dpp((int)mv, 0xAA, 0xf, 0xf, true), (u32)__builtin_amdgcn_mov_dpp((int)mv, 0xFF, 0xf, 0xf, true)};
      const u32 so = xb + X_MB + (((s + 1) & RM) * 2 + (jc >> 6)) * 16384;
      st128(rw, m4, (lane & 3) == 0 ? (jc & 63) * 128 + (lane & 31) * 4 + (lane >> 5) * 8192 : OOB - so, so);
    }
  }
}

DEVINL u64 uni64(const void* p) {
  const u64 a = (u64)p;
  return ((u64)(u32)__builtin_amdgcn_readfirstlane((int)(u32)(a >> 32)) << 32) | (u32)__builtin_amdgcn_readfirstlane((int)(u32)a);
}
DEVINL void glds16(u64 sbase, u32 voff, u32 ldsaddr) {
  u32 keep;
  asm volatile("s_mov_b32 %0, m0\n\ts_mov_b32 m0, %3\n\ts_nop 0\n\tglobal_load_lds_dwordx4 %1, %2\n\ts_mov_b32 m0, %0"
               : "=&s"(keep) : "v"(voff), "s"(sbase), "s"(ldsaddr) : "memory");
}
template <int N> DEVINL void vmcnt() { asm volatile("s_waitcnt vmcnt(%0)" ::"i"(N) : "memory"); }

template <int BM, int WM, int WN, int TI, int TJ, int NST, bool TRANS>
struct Gemm {
  static constexpr int XB = BM * 128, WB = 64 * 256, STB = XB + WB;
  static constexpr int GX = BM / 64, GW = 2, G = GX + GW;
  f32x16 acc[TI][TJ];
  DEVINL void run(const u16* X, int ldx, const u16* W, int ldw, int nk, char* smem, bool first) {
    const int tid = threadIdx.x, lane = tid & 63;
    const int w = __builtin_amdgcn_readfirstlane(tid >> 6);
    const int wm = w / WN, wn = w % WN;
    const u32 lds0 = __builtin_amdgcn_readfirstlane((u32)(size_t)smem);
    u32 xo[GX], wo[GW];
#pragma unroll
    for (int i = 0; i < GX; i++) {
      const int row = (w * GX + i) * 8 + (lane >> 3);
      xo[i] = (row * ldx + (((lane & 7) ^ ((row >> 1) & 7)) * 8)) * 2;
    }
#pragma unroll
    for (int i = 0; i < GW; i++) {
      const int kr = (w * GW + i) * 4 + (lane >> 4);
      wo[i] = (kr * ldw + (((lane & 15) ^ (((kr & 3) << 2) | ((kr >> 2) & 3))) * 8)) * 2;
    }
    const u64 X64 = uni64(X), W64 = uni64(W);
    const u32 wstep = __builtin_amdgcn_readfirstlane(ldw) * 128;
    auto piece = [&](int kt, int buf, int g) {
      if (g < GX) glds16(X64 + (u32)kt * 128, xo[g < GX ? g : 0], lds0 + buf * STB + (w * GX + g) * 1024);
      else glds16(W64 + (u64)((u32)kt * wstep), wo[g >= GX ? g - GX : 0], lds0 + buf * STB + XB + (w * GW + g - GX) * 1024);
    };
    auto stage = [&](int kt, int buf) {
#pragma unroll
      for (int g = 0; g < G; g++) piece(kt, buf, g);
    };
    const int hh = lane >> 5, m = lane & 31;
    const int tm = TRANS ? m : (16 * ((m >> 2) & 1) + 4 * (m >> 3) + (m & 3));
    u32 xa[4];
    {
      const int row = wm * TI * 32 + tm;
#pragma unroll
      for (int s = 0; s < 4; s++) xa[s] = row * 128 + (((2 * s + hh) ^ ((row >> 1) & 7)) * 16);
    }
    u32 wa[TJ][2];
    {
      const int blk = (lane >> 4) & 1, q = (lane & 15) >> 2, pp = lane & 3;
      const int ch = TRANS ? (2 * (pp & 1) + blk) : (2 * blk + (pp >> 1));
      const int half = TRANS ? (pp >> 1) : (pp & 1);
#pragma unroll
      for (int j = 0; j < TJ; j++)
#pragma unroll
        for (int t = 0; t < 2; t++) {
          const int k = 8 * hh + 4 * t + q;
          wa[j][t] = XB + k * 256 + ((((wn * TJ + j) * 4 + ch) ^ ((q << 2) | (2 * hh + t))) * 16) + half * 8;
        }
    }
    if (first) {
#pragma unroll
      for (int i = 0; i < TI; i++)
#pragma unroll
        for (int j = 0; j < TJ; j++)
#pragma unroll
          for (int e = 0; e < 16; e++) acc[i][j][e] = 0.f;
    }
#pragma unroll
    for (int st = 0; st < NST - 1; st++) if (st < nk) stage(st, st);
    int buf = 0;
    for (int kt = 0; kt < nk; kt++) {
      if (kt + NST - 2 < nk) vmcnt<G * (NST - 2)>(); else vmcnt<0>();
      BAR();
      const bool more = kt + NST - 1 < nk;
      const int nbuf = buf == 0 ? NST - 1 : buf - 1;
      const char* sb = smem + buf * STB;
      u32x4 xf[2][TI], wf[2][TJ];
      auto frags = [&](int s, int d) {
#pragma unroll
        for (int j = 0; j < TJ; j++) {
          s16x4 lo = __builtin_amdgcn_ds_read_tr16_b64_v4i16((lds_s16x4)(sb + wa[j][0] + s * 4096));
          s16x4 hi = __builtin_amdgcn_ds_read_tr16_b64_v4i16((lds_s16x4)(sb + wa[j][1] + s * 4096));
          wf[d][j] = __builtin_bit_cast(u32x4, __builtin_shufflevector(lo, hi, 0, 1, 2, 3, 4, 5, 6, 7));
        }
#pragma unroll
        for (int i = 0; i < TI; i++) xf[d][i] = *(const u32x4*)(sb + xa[s] + i * 4096);
      };
      frags(0, 0);
#pragma unroll
      for (int s = 0; s < 4; s++) {
        if (s < 3) frags(s + 1, (s + 1) & 1);
        SCHED0();
        constexpr int PP = (G + 3) / 4;
#pragma unroll
        for (int i = 0; i < TI; i++)
#pragma unroll
          for (int j = 0; j < TJ; j++) {
            acc[i][j] = TRANS ? mfma32(wf[s & 1][j], xf[s & 1][i], acc[i][j]) : mfma32(xf[s & 1][i], wf[s & 1][j], acc[i][j]);
            const int q = i * TJ + j, g = s * PP + q;
            if (q < PP && g < G) { SCHED0(); if (more) piece(kt + NST - 1, nbuf, g); SCHED0(); }
          }
        SCHED0();
      }
      buf = buf == NST - 1 ? 0 : buf + 1;
    }
    BAR();
  }
};

DEVINL float mish(float hv) {
  const float u = __expf(-fabsf(hv));
  const float num = hv > 0.f ? 1.f + 2.f * u : u * (u + 2.f);
  const float den = hv > 0.f ? num + 2.f * u * u : num + 2.f;
  return hv * num / den;
}

DEVINL void job_done(rsrc_t rw, int word) {
  VMCNT0();
  BAR();
  if (uni(threadIdx.x >> 6) == 0) __builtin_amdgcn_raw_ptr_buffer_atomic_add_i32(1, rw, lane0off(WS_CT + word * 4), 0, SC1);
}
DEVINL void job_wait(rsrc_t rw, int word, u32 target, int word2, u32 target2) {
  if (uni(threadIdx.x >> 6) == 0) {
    wait_ctr(rw, word, target);
    if (word2 >= 0) wait_ctr(rw, word2, target2);
    __builtin_amdgcn_fence(__ATOMIC_ACQUIRE, "agent");
    VMCNT0();
  }
  BAR();
}

DEVINL void job_proj(const Params& p, rsrc_t rw, char* smem, int kind, int rb, int ct) {
  const int tid = threadIdx.x, lane = tid & 63, w = tid >> 6, wm = w >> 1, wn = w & 1, hh = lane >> 5, m = lane & 31;
  const u16* X = (const u16*)(p.ws + WS_XN) + (size_t)rb * 256 * D_;
  if (kind == 1) {
    Gemm<256, 4, 2, 2, 2, 3, false> gm;
    gm.run(X, D_, p.wv + ct * 128, HD, 16, smem, true);
#pragma unroll
    for (int i = 0; i < 2; i++)
#pragma unroll
      for (int j = 0; j < 2; j++) {
        const int seg = rb * 8 + wm * 2 + i, e = wn * 64 + j * 32 + m;
        const u32 o = (((seg * 32 + ct) * 128 + e) * 32 + 16 * hh) * 2;
        const f32x16 a = gm.acc[i][j];
        st128(rw, u32x4{cvtpk(a[0], a[1]), cvtpk(a[2], a[3]), cvtpk(a[4], a[5]), cvtpk(a[6], a[7])}, o, WS_VT);
        st128(rw, u32x4{cvtpk(a[8], a[9]), cvtpk(a[10], a[11]), cvtpk(a[12], a[13]), cvtpk(a[14], a[15])}, o + 16, WS_VT);
      }
    job_done(rw, CT_KV + rb * 32);
  } else {
    Gemm<256, 4, 2, 2, 2, 3, true> gm;
    const u16* W = kind == 0 ? p.wk : kind == 2 ? p.wq : p.w1;
    const int ldw = kind == 3 ? F_ : HD;
    gm.run(X, D_, W + ct * 128, ldw, 16, smem, true);
    const u32 base = kind == 0 ? WS_KX : kind == 2 ? WS_QX : WS_ACT;
#pragma unroll
    for (int j = 0; j < 2; j++) {
      const int n0 = ct * 128 + wn * 64 + j * 32 + 16 * hh;
      float bias[16];
      if (kind == 3) {
        const u32x4 b0 = *(const u32x4*)(p.b1 + n0), b1 = *(const u32x4*)(p.b1 + n0 + 8);
#pragma unroll
        for (int e = 0; e < 4; e++) { bias[2 * e] = bflo(b0[e]); bias[2 * e + 1] = bfhi(b0[e]); bias[8 + 2 * e] = bflo(b1[e]); bias[9 + 2 * e] = bfhi(b1[e]); }
      }
#pragma unroll
      for (int i = 0; i < 2; i++) {
        const int tok = rb * 256 + wm * 64 + i * 32 + m;
        f32x16 a = gm.acc[i][j];
        if (kind == 3) {
#pragma unroll
          for (int e = 0; e < 16; e++) a[e] = mish(a[e] + bias[e]);
        }
        const u32 o = (tok * ldw + n0) * 2;
        st128(rw, u32x4{cvtpk(a[0], a[1]), cvtpk(a[2], a[3]), cvtpk(a[4], a[5]), cvtpk(a[6], a[7])}, o, base);
        st128(rw, u32x4{cvtpk(a[8], a[9]), cvtpk(a[10], a[11]), cvtpk(a[12], a[13]), cvtpk(a[14], a[15])}, o + 16, base);
      }
    }
    job_done(rw, (kind == 0 ? CT_KV : kind == 2 ? CT_Q : CT_F1) + rb * 32);
  }
}

DEVINL void job_kv0(const Params& p, rsrc_t rw, char* smem, int kind, int rb, int ct) {
  const int tid = threadIdx.x, lane = tid & 63, w = tid >> 6, wm = w >> 2, wn = w & 3, hh = lane >> 5, m = lane & 31;
  const u16* X = (const u16*)(p.ws + WS_XN) + (size_t)rb * 64 * D_;
  f32x16 a;
  u32 o;
  if (kind == 1) {
    Gemm<64, 2, 4, 1, 1, 6, false> gm;
    gm.run(X, D_, p.wv + ct * 128, HD, 16, smem, true);
    a = gm.acc[0][0];
    o = WS_VT + ((((rb * 2 + wm) * 32 + ct) * 128 + wn * 32 + m) * 32 + 16 * hh) * 2;
  } else {
    Gemm<64, 2, 4, 1, 1, 6, true> gm;
    gm.run(X, D_, p.wk + ct * 128, HD, 16, smem, true);
    a = gm.acc[0][0];
    o = WS_KX + ((rb * 64 + wm * 32 + m) * HD + ct * 128 + wn * 32 + 16 * hh) * 2;
  }
  st128(rw, u32x4{cvtpk(a[0], a[1]), cvtpk(a[2], a[3]), cvtpk(a[4], a[5]), cvtpk(a[6], a[7])}, o, 0);
  st128(rw, u32x4{cvtpk(a[8], a[9]), cvtpk(a[10], a[11]), cvtpk(a[12], a[13]), cvtpk(a[14], a[15])}, o + 16, 0);
  job_done(rw, CT_KV0 + rb * 32);
}

DEVINL void job_ffn2(const Params& p, rsrc_t rw, char* smem, int rb, int ct) {
  const int tid = threadIdx.x, lane = tid & 63, w = tid >> 6, wm = w >> 1, wn = w & 1, hh = lane >> 5, m = lane & 31;
  job_wait(rw, CT_F1 + rb * 32, 28, -1, 0);
  Gemm<256, 4, 2, 2, 2, 3, true> gm;
  gm.run((const u16*)(p.ws + WS_ACT) + (size_t)rb * 256 * F_, F_, p.w2 + ct * 128, D_, F_ / 64, smem, true);
#pragma unroll
  for (int i = 0; i < 2; i++)
#pragma unroll
    for (int j = 0; j < 2; j++) {
      const int tok = rb * 256 + wm * 64 + i * 32 + m, n0 = ct * 128 + wn * 64 + j * 32 + 16 * hh;
      const u32x4 x0 = *(const u32x4*)(p.x + tok * D_ + n0), x1 = *(const u32x4*)(p.x + tok * D_ + n0 + 8);
      const f32x16 a = gm.acc[i][j];
      const u32 o = (tok * D_ + n0) * 4;
#pragma unroll
      for (int q = 0; q < 4; q++) {
        const u32 xa = q < 2 ? x0[2 * q] : x1[2 * q - 4], xb = q < 2 ? x0[2 * q + 1] : x1[2 * q - 3];
        const f32x4 v = {a[4 * q] + bflo(xa), a[4 * q + 1] + bfhi(xa), a[4 * q + 2] + bflo(xb), a[4 * q + 3] + bfhi(xb)};
        st128(rw, __builtin_bit_cast(u32x4, v), o + q * 16, WS_Y);
      }
    }
  job_done(rw, CT_F2 + rb * 32);
}

DEVINL void job_wo(const Params& p, rsrc_t rw, char* smem, int rb, int ct) {
  const int tid = threadIdx.x, lane = tid & 63, w = tid >> 6, wm = w >> 2, wn = w & 3, hh = lane >> 5, m = lane & 31;
  job_wait(rw, CT_OX + rb * 32, 8, CT_F2 + (rb >> 2) * 32, 8);
  Gemm<64, 2, 4, 1, 1, 6, true> gm;
  gm.run((const u16*)(p.ws + WS_OX) + (size_t)rb * 64 * HD, HD, p.wo + ct * 128, D_, HD / 64, smem, true);
  const int tok = rb * 64 + wm * 32 + m, n0 = ct * 128 + wn * 32 + 16 * hh;
  const f32x4* y = (const f32x4*)(p.ws + WS_Y + (size_t)(tok * D_ + n0) * 4);
  const f32x16 a = gm.acc[0][0];
  u32 o[8];
#pragma unroll
  for (int q = 0; q < 4; q++) {
    const f32x4 yv = y[q];
    o[2 * q] = cvtpk(a[4 * q] + yv[0], a[4 * q + 1] + yv[1]);
    o[2 * q + 1] = cvtpk(a[4 * q + 2] + yv[2], a[4 * q + 3] + yv[3]);
  }
  u32x4* dst = (u32x4*)(p.out + tok * D_ + n0);
  dst[0] = u32x4{o[0], o[1], o[2], o[3]};
  dst[1] = u32x4{o[4], o[5], o[6], o[7]};
}

DEVINL void job_wo_sk(const Params& p, rsrc_t rw, char* smem, int rb, int ct, int ks) {
  const int tid = threadIdx.x, lane = tid & 63, w = tid >> 6, wm = w >> 2, wn = w & 3, hh = lane >> 5, m = lane & 31;
  const int g = (rb - (32 - NSK)) * 8 + ct;
  job_wait(rw, CT_OX + rb * 32, 8, CT_F2 + (rb >> 2) * 32, 8);
  Gemm<64, 2, 4, 1, 1, 6, true> gm;
  gm.run((const u16*)(p.ws + WS_OX) + (size_t)rb * 64 * HD + ks * 512, HD, p.wo + (size_t)ks * 512 * D_ + ct * 128, D_, 8, smem, true);
  const f32x16 a = gm.acc[0][0];
  const u32 so = WS_SL + (g * 8 + ks) * 32768 + ((wm * 32 + m) * 128 + wn * 32 + 16 * hh) * 4;
#pragma unroll
  for (int q = 0; q < 4; q++) st128(rw, __builtin_bit_cast(u32x4, f32x4{a[4 * q], a[4 * q + 1], a[4 * q + 2], a[4 * q + 3]}), so + q * 16, 0);
  job_done(rw, CT_SK + g * 32);
  if (uni(w) == 0) { wait_ctr(rw, CT_SK + g * 32, 8); }
  BAR();
  if (tid < 256) {
    const int row = ks * 8 + (tid >> 5), col = (tid & 31) * 4;
    const int tok = rb * 64 + row;
    f32x4 v = __builtin_bit_cast(f32x4, ld128(rw, (tok * D_ + ct * 128 + col) * 4, WS_Y));
#pragma unroll
    for (int k = 0; k < 8; k++) v += __builtin_bit_cast(f32x4, ld128(rw, (row * 128 + col) * 4, WS_SL + (g * 8 + k) * 32768));
    *(u32x2*)(p.out + tok * D_ + ct * 128 + col) = u32x2{cvtpk(v[0], v[1]), cvtpk(v[2], v[3])};
  }
}

DEVINL void att_wave(rsrc_t rw, int s, int h, int lane) {
  const int g = lane >> 4, n = lane & 15;
  f32x4 sc2[2][3];
#pragma unroll
  for (int a = 0; a < 2; a++)
#pragma unroll
    for (int b = 0; b < 3; b++) sc2[a][b] = f32x4{0, 0, 0, 0};
  u32x4 q0[4], q1[4];
#pragma unroll
  for (int t = 0; t < 4; t++) {
    const u32 col = (h * DH + 32 * t + 8 * g) * 2;
    q0[t] = ld128(rw, (32 * s + n) * HD * 2 + col, WS_QX); q1[t] = ld128(rw, (32 * s + 16 + n) * HD * 2 + col, WS_QX);
    const u32x4 k0 = ld128(rw, (32 * s + n) * HD * 2 + col, WS_KX), k1 = ld128(rw, (32 * s + 16 + n) * HD * 2 + col, WS_KX);
    sc2[0][0] = mfma16(k0, q0[t], sc2[0][0]); sc2[0][1] = mfma16(k1, q0[t], sc2[0][1]);
    sc2[1][0] = mfma16(k0, q1[t], sc2[1][0]); sc2[1][1] = mfma16(k1, q1[t], sc2[1][1]);
  }
  u32x4 vx[8];
#pragma unroll
  for (int T = 0; T < 8; T++) {
    const int e = 32 * (T >> 1) + 8 * (n >> 2) + 4 * (T & 1) + (n & 3);
    const u32 vo = (((s * 32 + h) * 128 + e) * 32 + 4 * g) * 2;
    const u32x2 lo = ld64(rw, vo, WS_VT), hi = ld64(rw, vo + 32, WS_VT);
    vx[T] = u32x4{lo[0], lo[1], hi[0], hi[1]};
  }
  asm volatile("" : "+v"(vx[0]), "+v"(vx[1]), "+v"(vx[2]), "+v"(vx[3]), "+v"(vx[4]), "+v"(vx[5]), "+v"(vx[6]), "+v"(vx[7]));
  if (uni(threadIdx.x >> 6) == 0) {
    u32 spins = 0;
    for (;;) {
      CBAR();
      const u32 f = ld32(rw, WS_CT + (CT_MF + (s * 32 + (h & ~7)) * 4 + (lane & 31)) * 4, 0);
      if (__all(f != 0)) break;
      if ((++spins & 255) == 0) {
        const u32 t = uni(ld32(rw, WS_CT + CT_TMO * 4, 0));
        if (t != 0 || spins > SPIN_CAP) { st32(rw, 1u, lane0off(WS_CT + CT_TMO * 4), 0); break; }
      }
      __builtin_amdgcn_s_sleep(6);
    }
  }
  BAR();
  u32x4 km[4];
  u32x2 vm[8];
#pragma unroll
  for (int t = 0; t < 4; t++) km[t] = ld128(rw, (((s * 32 + h) * 8 + (n & 7)) * 128 + 32 * t + 8 * g) * 2, WS_KM);
#pragma unroll
  for (int T = 0; T < 8; T++) {
    const int e = 32 * (T >> 1) + 8 * (n >> 2) + 4 * (T & 1) + (n & 3);
    vm[T] = ld64(rw, ((s * 32 + h) * 128 + e) * 16 + (g & 1) * 8, WS_VM);
  }
  SCHED0();
#pragma unroll
  for (int t = 0; t < 4; t++) { sc2[0][2] = mfma16(km[t], q0[t], sc2[0][2]); sc2[1][2] = mfma16(km[t], q1[t], sc2[1][2]); }
  const float sc = 0.08838834764831845f * 1.4426950408889634f;
  u32x4 px[2]; u32x2 pm[2];
#pragma unroll
  for (int a = 0; a < 2; a++) {
    const int qq = 16 * a + n;
    const float ninf = -__builtin_inff();
    float v[12];
#pragma unroll
    for (int i = 0; i < 4; i++) {
      v[i] = (4 * g + i <= qq) ? sc2[a][0][i] : ninf;
      v[4 + i] = (16 + 4 * g + i <= qq) ? sc2[a][1][i] : ninf;
      v[8 + i] = g < 2 ? sc2[a][2][i] : ninf;
    }
    float mx = max3(max3(v[0], v[1], v[2]), max3(v[3], v[4], v[5]), max3(v[6], v[7], v[8]));
    mx = xg_max(max3(mx, max3(v[9], v[10], v[11]), mx));
    const float mo = mx * sc;
    float sum = 0.f;
#pragma unroll
    for (int i = 0; i < 12; i++) { v[i] = __builtin_amdgcn_exp2f(v[i] * sc - mo); sum += v[i]; }
    sum = xg_sum(sum);
    const float inv = 1.0f / sum;
    px[a] = u32x4{cvtpk(v[0] * inv, v[1] * inv), cvtpk(v[2] * inv, v[3] * inv), cvtpk(v[4] * inv, v[5] * inv), cvtpk(v[6] * inv, v[7] * inv)};
    pm[a] = u32x2{cvtpk(v[8] * inv, v[9] * inv), cvtpk(v[10] * inv, v[11] * inv)};
  }
#pragma unroll
  for (int u = 0; u < 4; u++) {
    f32x4 o[2][2];
#pragma unroll
    for (int T = 0; T < 2; T++) {
      const u32x2 vmz = g >= 2 ? u32x2{0, 0} : vm[2 * u + T];
      const f32x4 z = {0, 0, 0, 0};
      o[0][T] = mfma16k16(vmz, pm[0], mfma16(vx[2 * u + T], px[0], z));
      o[1][T] = mfma16k16(vmz, pm[1], mfma16(vx[2 * u + T], px[1], z));
    }
#pragma unroll
    for (int a = 0; a < 2; a++) {
      const u32x4 ov = {cvtpk(o[a][0][0], o[a][0][1]), cvtpk(o[a][0][2], o[a][0][3]), cvtpk(o[a][1][0], o[a][1][1]), cvtpk(o[a][1][2], o[a][1][3])};
      st128(rw, ov, ((32 * s + 16 * a + n) * HD + h * DH + 32 * u + 8 * g) * 2, WS_OX);
    }
  }
}

constexpr int NJ_KV0 = 256, NJ_KV = 448, NJ_F1 = 224, NJ_Q = 256, NJ_F2 = 64, NJ_TAIL = (32 - NSK) * 16, NJ_SK = NSK * 72;
constexpr int J_KV = NJ_KV0, J_F1 = J_KV + NJ_KV, J_Q = J_F1 + NJ_F1, J_F2 = J_Q + NJ_Q, J_TAIL = J_F2 + NJ_F2, J_SK = J_TAIL + NJ_TAIL,
              J_END = J_SK + NJ_SK;

DEVINL void job_att(const Params& p, rsrc_t rw, int rb, int k) {
  const int s = rb * 2 + (k >> 2), h = (k & 3) * 8 + (threadIdx.x >> 6);
  if (s < 8) job_wait(rw, CT_KV0 + (s >> 1) * 32, 64, CT_Q, 32);
  else job_wait(rw, CT_KV + (s >> 3) * 32, 64, CT_Q + (s >> 3) * 32, 32);
  att_wave(rw, s, h, threadIdx.x & 63);
  job_done(rw, CT_OX + rb * 32);
}

DEVINL void worker_role(const Params& p, char* smem) {
  const rsrc_t rw = mkrsrc(p.ws, WS_END);
  int* sjob = (int*)(smem + 147456);
  bool prep_seen = false;
  for (;;) {
    if (uni(threadIdx.x >> 6) == 0) {
      const int t = __builtin_amdgcn_raw_ptr_buffer_atomic_add_i32(1, rw, lane0off(WS_CT + CT_TICKET * 4), 0, SC1);
      *sjob = __builtin_amdgcn_readfirstlane(t);
    }
    BAR();
    const int j = __builtin_amdgcn_readfirstlane(*sjob);
    BAR();
    WSTAMP(j < J_END ? 2 * j : 2 * J_END + blockIdx.x);
    if (j >= J_END) break;
    if (!prep_seen && j < J_F2) { job_wait(rw, CT_PREP, 128, -1, 0); prep_seen = true; }
    if (j < J_KV) {
      job_kv0(p, rw, smem, (j >> 5) & 1, j >> 6, j & 31);
    } else if (j < J_F2) {
      int kind, rb, ct;
      if (j < J_F1) { const int i = j - J_KV; rb = 1 + (i >> 6); ct = i & 31; kind = (i >> 5) & 1; }
      else if (j < J_Q) { const int i = j - J_F1; rb = i / 28; ct = i % 28; kind = 3; }
      else { const int i = j - J_Q; rb = i >> 5; ct = i & 31; kind = 2; }
      job_proj(p, rw, smem, kind, rb, ct);
    } else if (j < J_TAIL) {
      const int i = j - J_F2;
      job_ffn2(p, rw, smem, i >> 3, i & 7);
    } else if (j < J_SK) {
      const int i = j - J_TAIL, rb = i >> 4, k = i & 15;
      if (k < 8) job_att(p, rw, rb, k);
      else job_wo(p, rw, smem, rb, k - 8);
    } else {
      const int i = j - J_SK, rb = 32 - NSK + i / 72, k = i % 72;
      if (k < 8) job_att(p, rw, rb, k);
      else job_wo_sk(p, rw, smem, rb, (k - 8) >> 3, (k - 8) & 7);
    }
    WSTAMP(2 * j + 1);
  }
}

constexpr int LDS_BYTES = 147456 + 64;

__global__ __launch_bounds__(512, 2) void main_kernel(Params p) {
  extern __shared__ __attribute__((aligned(16))) char smem[];
  if (blockIdx.x < 128) {
    rec_role(p, smem, blockIdx.x);
    __syncthreads();
  } else {
    const rsrc_t rw = mkrsrc(p.ws, WS_END);
    prep_role(p, rw, blockIdx.x - 128);
    job_done(rw, CT_PREP);
  }
  worker_role(p, smem);
}

extern "C" void kernel_launch(void* const* d_in, const int* in_sizes, int n_in, void* d_out, int out_size, void* d_ws,
                              size_t ws_size, hipStream_t stream) {
  Params p;
  p.x = (const u16*)d_in[0];
  p.mem0 = (const u16*)d_in[1];
  p.wq = (const u16*)d_in[2];
  p.wk = (const u16*)d_in[3];
  p.wv = (const u16*)d_in[4];
  p.wo = (const u16*)d_in[5];
  p.w1 = (const u16*)d_in[8];
  p.b1 = (const u16*)d_in[9];
  p.w2 = (const u16*)d_in[10];
  p.out = (u16*)d_out;
  p.ws = (char*)d_ws;
  hipFuncSetAttribute((const void*)main_kernel, hipFuncAttributeMaxDynamicSharedMemorySize, LDS_BYTES);
  hipMemsetAsync(p.ws + WS_CT, 0, CT_BYTES, stream);
  main_kernel<<<256, 512, LDS_BYTES, stream>>>(p);
}
```

```cpp
#include <hip/hip_runtime.h>
#include <hip/hip_bf16.h>
#include <stdint.h>

typedef unsigned int u32;
typedef unsigned short u16;
typedef unsigned long long u64;
typedef __attribute__((ext_vector_type(4))) u32 u32x4;
typedef __attribute__((ext_vector_type(2))) u32 u32x2;
typedef __attribute__((ext_vector_type(4))) float f32x4;
typedef __attribute__((ext_vector_type(2))) float f32x2;
typedef __attribute__((ext_vector_type(16))) float f32x16;
typedef __attribute__((ext_vector_type(8))) __bf16 bf16x8;
typedef __attribute__((ext_vector_type(4))) short s16x4;
typedef __attribute__((ext_vector_type(8))) short s16x8;
typedef __attribute__((address_space(3))) s16x4* lds_s16x4;

typedef __attribute__((address_space(3))) volatile int* lds_vint;
typedef __attribute__((address_space(3))) volatile u32x4* lds_vu32x4;

#define DEVINL __device__ __forceinline__
constexpr int S_ = 2048, D_ = 1024, H_ = 32, DH = 128, HD = 4096, F_ = 3584, NSEG = 64;
constexpr u32 WS_XN = 0;
constexpr u32 WS_QX = WS_XN + (4u << 20);
constexpr u32 WS_KX = WS_QX + (16u << 20);
constexpr u32 WS_VT = WS_KX + (16u << 20);
constexpr u32 WS_ACT = WS_VT + (16u << 20);
constexpr u32 WS_Y = WS_ACT + (14u << 20);
constexpr u32 WS_OX = WS_Y + (8u << 20);
constexpr u32 WS_KM = WS_OX + (16u << 20);
constexpr u32 WS_VM = WS_KM + (4u << 20);
constexpr u32 WS_CT = WS_VM + (4u << 20);
constexpr u32 CT_BYTES = 64u << 10;
constexpr u32 WS_SL = WS_CT + CT_BYTES;
constexpr int NSK = 2;
constexpr u32 WS_X = WS_SL + NSK * 64 * 32768;
constexpr int RING = 4, RM = RING - 1;
constexpr u32 X_MB = 0;
constexpr u32 X_OB = X_MB + RING * 2 * 16384;
constexpr u32 X_SB = X_OB + RING * 8 * 16384;
constexpr u32 X_BYTES = X_SB + RING * 32 * 16384;
constexpr u32 WS_END = WS_X + X_BYTES + 16384;
DEVINL u32 xbase(const char* ws) { return WS_X + ((16384u - (u32)(((u64)ws + WS_X) & 16383u)) & 16383u); }
constexpr int CT_TICKET = 0, CT_TMO = 32, CT_KV = 64, CT_Q = CT_KV + 8 * 32, CT_F1 = CT_Q + 8 * 32, CT_F2 = CT_F1 + 8 * 32,
              CT_OX = CT_F2 + 8 * 32, CT_KV0 = CT_OX + 32 * 32, CT_PREP = CT_KV0 + 4 * 32, CT_SK = CT_PREP + 32, CT_RA = CT_SK + NSK * 8 * 32, CT_PAR = CT_RA + 32  ,
              CT_CI = CT_PAR + 32,
              CT_MF = 4096;
constexpr u32 SENT = 0xFFFFFFFFu;
constexpr u32 SPIN_CAP = 1u << 18;

struct Params {
  const u16 *x, *mem0, *wq, *wk, *wv, *wo, *w1, *b1, *w2;
  u16* out;
  char* ws;
};

DEVINL float bf2f(u16 v) { return __uint_as_float((u32)v << 16); }
DEVINL float bflo(u32 v) { return __uint_as_float(v << 16); }
DEVINL float bfhi(u32 v) { return __uint_as_float(v & 0xFFFF0000u); }
DEVINL u32 cvtpk(float lo, float hi) {
  typedef __attribute__((ext_vector_type(2))) __bf16 bf16x2;
  bf16x2 r = {(__bf16)lo, (__bf16)hi};
  return __builtin_bit_cast(u32, r);
}
DEVINL float rbf(float v) { return bflo(cvtpk(v, v)); }
DEVINL u32 nosent(u32 v) { return v < 0xFFFFFFFEu ? v : 0xFFFFFFFEu; }
DEVINL f32x4 mfma16(u32x4 a, u32x4 b, f32x4 c) {
  return __builtin_amdgcn_mfma_f32_16x16x32_bf16(__builtin_bit_cast(bf16x8, a), __builtin_bit_cast(bf16x8, b), c, 0, 0, 0);
}
DEVINL f32x4 mfma16k16(u32x2 a, u32x2 b, f32x4 c) {
  return mfma16(u32x4{a[0], a[1], 0, 0}, u32x4{b[0], b[1], 0, 0}, c);
}
DEVINL f32x16 mfma32(u32x4 a, u32x4 b, f32x16 c) {
  return __builtin_amdgcn_mfma_f32_32x32x16_bf16(__builtin_bit_cast(bf16x8, a), __builtin_bit_cast(bf16x8, b), c, 0, 0, 0);
}
DEVINL float max3(float a, float b, float c) { float r; asm("v_max3_f32 %0, %1, %2, %3" : "=v"(r) : "v"(a), "v"(b), "v"(c)); return r; }
DEVINL float max2(float a, float b) { float r; asm("v_max_f32 %0, %1, %2" : "=v"(r) : "v"(a), "v"(b)); return r; }
DEVINL float xg_max(float x) {
  auto s = __builtin_amdgcn_permlane16_swap(__float_as_uint(x), __float_as_uint(x), false, false);
  x = max2(__uint_as_float(s[0]), __uint_as_float(s[1]));
  auto t = __builtin_amdgcn_permlane32_swap(__float_as_uint(x), __float_as_uint(x), false, false);
  return max2(__uint_as_float(t[0]), __uint_as_float(t[1]));
}
DEVINL float xg_sum(float x) {
  auto s = __builtin_amdgcn_permlane16_swap(__float_as_uint(x), __float_as_uint(x), false, false);
  x = __uint_as_float(s[0]) + __uint_as_float(s[1]);
  auto t = __builtin_amdgcn_permlane32_swap(__float_as_uint(x), __float_as_uint(x), false, false);
  return __uint_as_float(t[0]) + __uint_as_float(t[1]);
}
typedef __amdgpu_buffer_rsrc_t rsrc_t;
DEVINL rsrc_t mkrsrc(const void* p, u32 n) { return __builtin_amdgcn_make_buffer_rsrc((void*)p, 0, n, 0x00020000); }
constexpr int SC1 = 16;
DEVINL u32x4 ld128(rsrc_t r, u32 vo, u32 so) { return __builtin_amdgcn_raw_buffer_load_b128(r, vo, so, SC1); }
DEVINL u32x2 ld64(rsrc_t r, u32 vo, u32 so) { return __builtin_amdgcn_raw_buffer_load_b64(r, vo, so, SC1); }
DEVINL u32 ld32(rsrc_t r, u32 vo, u32 so) { return __builtin_amdgcn_raw_buffer_load_b32(r, vo, so, SC1); }
DEVINL void st128(rsrc_t r, u32x4 v, u32 vo, u32 so) { __builtin_amdgcn_raw_buffer_store_b128(v, r, vo + so, 0, SC1); }
DEVINL void st64(rsrc_t r, u32x2 v, u32 vo, u32 so) { __builtin_amdgcn_raw_buffer_store_b64(v, r, vo, so, SC1); }
DEVINL void st32(rsrc_t r, u32 v, u32 vo, u32 so) { __builtin_amdgcn_raw_buffer_store_b32(v, r, vo, so, SC1); }
#define CBAR() asm volatile("" ::: "memory")
#define VMCNT0() asm volatile("s_waitcnt vmcnt(0)" ::: "memory")
#define BAR() do { asm volatile("s_waitcnt lgkmcnt(0)" ::: "memory"); __builtin_amdgcn_s_barrier(); CBAR(); } while (0)
DEVINL bool isnan4(f32x4 v) { float s = (v[0] + v[1]) + (v[2] + v[3]); return s != s; }
DEVINL u32 umax(u32 a, u32 b) { return a > b ? a : b; }
DEVINL u32 max4(u32x4 v) { return umax(umax(v[0], v[1]), umax(v[2], v[3])); }
#define SCHED0() __builtin_amdgcn_sched_barrier(0)
DEVINL void nap(int n) { for (int i = 0; i < n; i++) __builtin_amdgcn_s_sleep(1); }

constexpr u32 OOB = 0x7FFFFFF0u;
DEVINL u32 lane0off(u32 off) { return (threadIdx.x & 63) == 0 ? off : OOB; }
DEVINL u32 uni(u32 v) { return (u32)__builtin_amdgcn_readfirstlane((int)v); }
DEVINL void wait_ctr(rsrc_t rw, int word, u32 target) {
  u32 spins = 0;
  for (;;) {
    CBAR();
    if (uni(ld32(rw, WS_CT + word * 4, 0)) >= target) break;
    if ((++spins & 255) == 0) {
      const u32 t = uni(ld32(rw, WS_CT + CT_TMO * 4, 0));
      if (t != 0 || spins > SPIN_CAP) { st32(rw, 1u, lane0off(WS_CT + CT_TMO * 4), 0); break; }
    }
    __builtin_amdgcn_s_sleep(4);
  }
}

DEVINL bool spin_fail(rsrc_t rw, u32& spins) {
  if ((++spins & 63) != 0) return false;
  const u32 t = ld32(rw, WS_CT + CT_TMO * 4, 0);
  if (uni(t) == 0 && spins < SPIN_CAP) return false;
  st32(rw, 1u, lane0off(WS_CT + CT_TMO * 4), 0);
  return true;
}

DEVINL void prep_role(const Params& p, rsrc_t rw, int wb) {
  const int tid = threadIdx.x, lane = tid & 63, w = tid >> 6;
#pragma unroll
  for (int rr = 0; rr < 2; rr++) {
    const int row = wb * 16 + w * 2 + rr;
    const u32x4* xr = (const u32x4*)(p.x + (size_t)row * D_);
    u32x4 a = xr[lane], b = xr[64 + lane];
    float v[16];
#pragma unroll
    for (int i = 0; i < 4; i++) { v[2 * i] = bflo(a[i]); v[2 * i + 1] = bfhi(a[i]); v[8 + 2 * i] = bflo(b[i]); v[9 + 2 * i] = bfhi(b[i]); }
    float s = 0.f;
#pragma unroll
    for (int i = 0; i < 16; i++) s += v[i];
#pragma unroll
    for (int o = 32; o > 0; o >>= 1) s += __shfl_xor(s, o);
    const float mean = s * (1.0f / D_);
    float q = 0.f;
#pragma unroll
    for (int i = 0; i < 16; i++) { float d = v[i] - mean; q += d * d; }
#pragma unroll
    for (int o = 32; o > 0; o >>= 1) q += __shfl_xor(q, o);
    const float mb = rbf(mean), den = rbf(sqrtf(rbf(rbf(q * (1.0f / D_)) + 1e-5f)));
    u32x4 oa, ob;
#pragma unroll
    for (int i = 0; i < 4; i++) {
      oa[i] = cvtpk(rbf(v[2 * i] - mb) / den, rbf(v[2 * i + 1] - mb) / den);
      ob[i] = cvtpk(rbf(v[8 + 2 * i] - mb) / den, rbf(v[9 + 2 * i] - mb) / den);
    }
    st128(rw, oa, row * 2048 + lane * 16, WS_XN);
    st128(rw, ob, row * 2048 + 1024 + lane * 16, WS_XN);
  }
  const u32 xb = xbase(p.ws);
  const u32x4 sv = {SENT, SENT, SENT, SENT};
  constexpr u32 NS = RING;
  for (u32 i = wb * 512 + tid; i < (NS - 1) * 2 * 1024; i += 128 * 512) st128(rw, sv, i * 16, xb + X_MB + 2 * 16384);
  for (u32 i = wb * 512 + tid; i < NS * 8 * 1024; i += 128 * 512) st128(rw, sv, i * 16, xb + X_OB);
  for (u32 i = wb * 512 + tid; i < NS * 32 * 640; i += 128 * 512) {
    const u32 pr = i / 640, k = i - pr * 640;
    st128(rw, sv, pr * 16384 + (k >= 320 ? 8192 + (k - 320) * 16 : k * 16), xb + X_SB);
  }
  if (wb < 2) {
    const int i = wb * 512 + tid;
    const u32x4 v = *(const u32x4*)(p.mem0 + (i & 7) * D_ + (i >> 3) * 8);
    st128(rw, v, tid * 16, xb + X_MB + wb * 16384);
    st128(rw, v, tid * 16 + 8192, xb + X_MB + wb * 16384);
  }
}

constexpr int L_QK = 0, L_V = 8192, L_O = 16384, L_KV = 20480, L_VM = 28672, L_VF = 29696;
constexpr int D_E1X = 4;
constexpr int D_E1 = 11;
constexpr int D_E2 = 1;
constexpr int D_E3 = 7;

DEVINL void rec_prefetch(rsrc_t rw, char* smem, int s, int h, int c, int lane) {
  const int g = lane >> 4, m = lane & 15;
  if (s < 8) { if ((s & 1) == 0) { wait_ctr(rw, CT_KV0 + (s >> 1) * 32, 64); CBAR(); } }
  else if ((s & 7) == 0) { wait_ctr(rw, CT_KV + (s >> 3) * 32, 64); CBAR(); }
  u32x4 k0 = ld128(rw, ((32 * s + m) * HD + h * DH + c * 32 + 8 * g) * 2, WS_KX);
  u32x4 k1 = ld128(rw, ((32 * s + 16 + m) * HD + h * DH + c * 32 + 8 * g) * 2, WS_KX);
  u32x4 v[2];
#pragma unroll
  for (int T = 0; T < 2; T++) {
    u32 o = (((s * 32 + h) * 128 + c * 32 + 16 * T + m) * 32 + 4 * g) * 2;
    u32x2 lo = ld64(rw, o, WS_VT), hi = ld64(rw, o + 32, WS_VT);
    v[T] = u32x4{lo[0], lo[1], hi[0], hi[1]};
  }
  u32x4* dst = (u32x4*)(smem + L_KV + (s & 1) * 4096);
  dst[lane] = k0; dst[64 + lane] = k1; dst[128 + lane] = v[0]; dst[192 + lane] = v[1];
}

DEVINL void rec_role(const Params& p, char* smem, int r) {
  const int tid = threadIdx.x, lane0 = tid & 63;
  const int w = __builtin_amdgcn_readfirstlane(tid >> 6);
  const int h = (r & 7) + 8 * (r >> 5), c = (r >> 3) & 3, jc = (r & 7) * 16 + (r >> 3);
  const int nh = w >> 2, kq = w & 3;
  const rsrc_t rw = mkrsrc(p.ws, WS_END);
  const u32 xb = xbase(p.ws);
  const u32 pg0 = (u32)(((u64)p.ws + xb) >> 21);
  const int npg = (int)((u32)(((u64)p.ws + xb + X_BYTES - 1) >> 21) - pg0) + 1;
  u32 xcc;
  asm volatile("s_getreg_b32 %0, hwreg(HW_REG_XCC_ID, 0, 4)" : "=s"(xcc));
  const u32 iod = (xcc >> 2) & 1;
  {
    const int pgr = r % npg;
    u32 o = (u32)((((u64)(pg0 + pgr)) << 21) - (u64)p.ws);
    o = (o < xb || o > 0x80000000u) ? xb : o;
    o = xb + ((o - xb + 16383u) & ~16383u);
    if (o + 16384 > xb + X_BYTES) o -= 16384;
    {
      u32 lim = (u32)((((u64)(pg0 + pgr + 1)) << 21) - (u64)p.ws);
      lim = lim < xb + X_BYTES ? lim : xb + X_BYTES;
      const u32 np = lim > o + 16384 ? (lim - o) / 16384 : 1;
      o += ((u32)(r / npg) % np) * 16384;
    }
    {
      const char* a = p.ws + o + 7168;
      u32 v;
      asm volatile("global_load_dword %0, %1, off offset:-128 sc1\n\ts_waitcnt vmcnt(0)" : "=v"(v) : "v"(a) : "memory");
#pragma unroll
      for (int k = 0; k < 2; k++) {
        const int hf = (k ^ w) & 1;
        const char* b = a + hf * 8192 + w * 128;
        const u64 t0 = __builtin_amdgcn_s_memtime();
        asm volatile("s_waitcnt lgkmcnt(0)\n\tglobal_load_dword %0, %1, off sc1\n\ts_waitcnt vmcnt(0)" : "=v"(v) : "v"(b) : "memory");
        const u64 t1 = __builtin_amdgcn_s_memtime();
        *(lds_vint)(smem + L_V + w * 8 + hf * 4) = (int)(u32)(t1 - t0);
      }
    }
    BAR();
    if (w == 0) {
      u32 b0 = ~0u, b1 = ~0u;
      for (int k = 0; k < 8; k++) {
        const u32 x = (u32)*(lds_vint)(smem + L_V + k * 8), y = (u32)*(lds_vint)(smem + L_V + k * 8 + 4);
        b0 = x < b0 ? x : b0; b1 = y < b1 ? y : b1;
      }
      const u32 bit = (uni(b0) <= uni(b1) ? 0u : 1u) ^ iod;
      __builtin_amdgcn_raw_ptr_buffer_atomic_add_i32((int)bit, rw, lane0off(WS_CT + (CT_PAR + pgr) * 4), 0, SC1);
    }
  }
  if (w == 0) {
    if (c == 0) st32(rw, 1u + iod, lane0off(WS_CT + (CT_CI + h) * 4), 0);
    VMCNT0();
    __builtin_amdgcn_raw_ptr_buffer_atomic_add_i32(1, rw, lane0off(WS_CT + CT_RA * 4), 0, SC1);
  }
  u32x4 wqk[2][8], wv[8], wo[8];
  {
    const int lane = lane0, g = lane >> 4, q = (lane & 15) >> 2, pp = lane & 3;
    const u16* W = (nh ? p.wk : p.wq) + (size_t)(256 * kq) * HD + h * DH + c * 32;
    const u16* V = p.wv + (size_t)(256 * kq) * HD + h * DH + c * 32 + 16 * nh;
    const u16* O = p.wo + (size_t)(512 * w) * D_ + 8 * jc;
#define WLD(base, off) __builtin_amdgcn_raw_buffer_load_b128(mkrsrc(base, 0x7FFFFFFF), off, 0, 0)
    u32x4 ra[16], rb[8], rc[8];
#pragma unroll
    for (int i = 0; i < 16; i++) ra[i] = WLD(W, ((16 * i + (lane >> 2)) * HD + (lane & 3) * 8) * 2);
#pragma unroll
    for (int i = 0; i < 8; i++) rb[i] = WLD(V, ((32 * i + (lane >> 1)) * HD + (lane & 1) * 8) * 2);
#pragma unroll
    for (int i = 0; i < 8; i++) rc[i] = WLD(O, (64 * i + lane) * D_ * 2);
    SCHED0();
    SCHED0();
    char* ar = smem + w * 16384;
    BAR();
#pragma unroll
    for (int i = 0; i < 16; i++) *(u32x4*)(ar + i * 1024 + lane * 16) = ra[i];
    asm volatile("s_waitcnt lgkmcnt(0)" ::: "memory");
    auto tr = [&](const char* a0, const char* a1) -> u32x4 {
      const s16x4 lo = __builtin_amdgcn_ds_read_tr16_b64_v4i16((lds_s16x4)a0), hi = __builtin_amdgcn_ds_read_tr16_b64_v4i16((lds_s16x4)a1);
      return __builtin_bit_cast(u32x4, __builtin_shufflevector(lo, hi, 0, 1, 2, 3, 4, 5, 6, 7));
    };
#pragma unroll
    for (int t = 0; t < 8; t++)
#pragma unroll
      for (int T = 0; T < 2; T++) {
        const char* a = ar + (32 * t + 8 * g + q) * 64 + (16 * T + 4 * pp) * 2;
        wqk[T][t] = tr(a, a + 4 * 64);
      }
    asm volatile("s_waitcnt lgkmcnt(0)" ::: "memory");
    SCHED0();
#pragma unroll
    for (int i = 0; i < 8; i++) { *(u32x4*)(ar + i * 1024 + lane * 16) = rb[i]; *(u32x4*)(ar + 8192 + i * 1024 + lane * 16) = rc[i]; }
    asm volatile("s_waitcnt lgkmcnt(0)" ::: "memory");
#pragma unroll
    for (int t = 0; t < 8; t++) {
      const char* a = ar + (32 * t + 8 * g + q) * 32 + pp * 8;
      wv[t] = tr(a, a + 4 * 32);
      const char* b = ar + 8192 + (256 * (pp >> 1) + 32 * t + 8 * g + q) * 16 + (pp & 1) * 8;
      wo[t] = tr(b, b + 4 * 16);
    }
    asm volatile("s_waitcnt lgkmcnt(0)" ::: "memory");
  }
  VMCNT0();
  BAR();
  if (w == 0) {
    wait_ctr(rw, CT_RA, 128);
    wait_ctr(rw, CT_PREP, 128);
    CBAR();
    const u32 votes = ld32(rw, WS_CT + (CT_PAR + (lane0 & 31)) * 4, 0);
    const int voters = (128 - lane0 + npg - 1) / npg;
    const u32 pm = (u32)__ballot(lane0 < npg && 2 * (int)votes > voters), ci = uni(ld32(rw, WS_CT + (CT_CI + h) * 4, 0));
    *(lds_vint)(smem + L_VF + 16) = (int)pm;
    *(lds_vint)(smem + L_VF + 20) = (int)(ci - 1);
  }
  if (w == 7) rec_prefetch(rw, smem, 0, h, c, lane0);
  if (w == 1) *(lds_vint)(smem + L_VF) = 0;
  BAR();
  const u32 parmask = uni(*(lds_vint)(smem + L_VF + 16)), ciod = uni(*(lds_vint)(smem + L_VF + 20)) & 1;
  auto nearhalf = [&](u32 o, u32 d) -> u32 {
    const u32 pg = (u32)(((u64)p.ws + o) >> 21) - pg0;
    return o + ((((parmask >> pg) & 1u) ^ d) << 13);
  };
  const float scale = 0.08838834764831845f;
  const float l2e = 1.4426950408889634f;

  int dl1 = w == 0 ? D_E1 : D_E1 + D_E1X, dl2 = D_E2, dl3 = D_E3;
  const int d1min = dl1;
  auto adapt = [&](int& d, int dmin, u32 spins, int s) {
    if (spins != 0) d = d + 1 < dmin + 8 ? d + 1 : dmin + 8;
    else if ((s & 1) == 1 && d > dmin) d -= 1;
  };
  for (int s = 0; s < NSEG; s++) {
    int lane = lane0;
    asm volatile("" : "+v"(lane));
    const int g = lane >> 4, n = lane & 15;
    u32x4 mf[8];
    f32x4 d0, d1;
    {
      const u32 vo = ((kq & 1) * 32 + g) * 128 + (n & 7) * 16, so = nearhalf(xb + X_MB + ((s & RM) * 2 + (kq >> 1)) * 16384, iod);
      u32 spins = 0;
      if (s > 0) nap(dl1);
      for (;;) {
        CBAR();
#pragma unroll
        for (int t = 0; t < 8; t++) mf[t] = ld128(rw, vo, so + t * 512);
        SCHED0();
        d0 = f32x4{0, 0, 0, 0}; d1 = d0;
#pragma unroll
        for (int t = 0; t < 8; t++) { d0 = mfma16(wqk[0][t], mf[t], d0); d1 = mfma16(wqk[1][t], mf[t], d1); }
        if (!__any(isnan4(d0 + d1))) break;
        u32 mxw = 0;
#pragma unroll
        for (int t = 0; t < 8; t++) mxw = umax(mxw, max4(mf[t]));
        if (!__any(mxw == SENT)) break;
        if (spin_fail(rw, spins)) break;
      }
      adapt(dl1, d1min, spins, s);
    }
    {
      f32x4 dv = n < 8 ? d0 : d1;
      *(f32x4*)(smem + L_QK + kq * 2048 + nh * 1024 + (n >> 3) * 512 + g * 128 + (n & 7) * 16) = dv;
    }
    BAR();
    u32x4 F;
    u32x4 ea[4], eb[4], ec[4];
    const u32 sb = nearhalf(xb + X_SB + ((s & RM) * 32 + h) * 16384, ciod);
    auto e2_load = [&]() {
      const u32 vo = (n & 7) * 64 + g * 16, vm_o = 1024 + (n & 7) * 32 + (g & 1) * 16;
#pragma unroll
      for (int k = 0; k < 4; k++) {
        const u32 so = sb + k * 1280;
        ea[k] = ld128(rw, vo, so); eb[k] = ld128(rw, vo, so + 512); ec[k] = ld128(rw, vm_o, so);
      }
      SCHED0();
    };
    if (w == 0) {
      const char* b = smem + L_QK + (n >> 3) * 1024 + (g >> 1) * 512 + (g & 1) * 256 + (n & 7) * 16;
      f32x4 a0 = *(const f32x4*)(b), a1 = *(const f32x4*)(b + 128);
#pragma unroll
      for (int k = 1; k < 4; k++) { a0 += *(const f32x4*)(b + k * 2048); a1 += *(const f32x4*)(b + k * 2048 + 128); }
      F = u32x4{cvtpk(a0[0], a0[1]), cvtpk(a0[2], a0[3]), cvtpk(a1[0], a1[1]), cvtpk(a1[2], a1[3])};
      if (s < NSEG - 1) {
        const u32x4* kv = (const u32x4*)(smem + L_KV + (s & 1) * 4096);
        const f32x4 z = {0, 0, 0, 0};
        f32x4 sx0 = mfma16(kv[lane], F, z), sx1 = mfma16(kv[64 + lane], F, z), sm = mfma16(F, F, z);
        const u32 so = sb + c * 1280;
        if (n < 8) {
          st128(rw, __builtin_bit_cast(u32x4, sx0), n * 64 + g * 16, so);
          st128(rw, __builtin_bit_cast(u32x4, sx1), 512 + n * 64 + g * 16, so);
          if (g >= 2) st128(rw, __builtin_bit_cast(u32x4, sm), 1024 + n * 32 + (g - 2) * 16, so);
        }

      }
    }
    {
      f32x4 dv = {0, 0, 0, 0};
#pragma unroll
      for (int t = 0; t < 8; t++) dv = mfma16(mf[t], wv[t], dv);
      *(f32x4*)(smem + L_V + kq * 2048 + nh * 1024 + lane * 16) = dv;
    }
    BAR();
    if (w == 1) {
      u32x2 vm[2];
#pragma unroll
      for (int T = 0; T < 2; T++) {
        const char* b = smem + L_V + T * 1024 + lane * 16;
        f32x4 a = *(const f32x4*)(b);
#pragma unroll
        for (int k = 1; k < 4; k++) a += *(const f32x4*)(b + k * 2048);
        vm[T] = u32x2{cvtpk(a[0], a[1]), cvtpk(a[2], a[3])};
      }
      *(u32x4*)(smem + L_VM + lane * 16) = u32x4{vm[0][0], vm[0][1], vm[1][0], vm[1][1]};
      asm volatile("s_waitcnt lgkmcnt(0)" ::: "memory");
      *(lds_vint)(smem + L_VF) = s + 1;
      const char* b = smem + L_QK + 1024 + (g >> 1) * 512 + (g & 1) * 256 + (n & 7) * 16;
      f32x4 a0 = *(const f32x4*)(b), a1 = *(const f32x4*)(b + 128);
#pragma unroll
      for (int k = 1; k < 4; k++) { a0 += *(const f32x4*)(b + k * 2048); a1 += *(const f32x4*)(b + k * 2048 + 128); }
      const u32x4 km = {cvtpk(a0[0], a0[1]), cvtpk(a0[2], a0[3]), cvtpk(a1[0], a1[1]), cvtpk(a1[2], a1[3])};
      if (n < 8) st128(rw, km, (((s * 32 + h) * 8 + n) * 128 + c * 32 + 8 * g) * 2, WS_KM);
#pragma unroll
      for (int T = 0; T < 2; T++) {
        auto s0 = __builtin_amdgcn_permlane16_swap(vm[T][0], vm[T][0], false, false);
        auto s1 = __builtin_amdgcn_permlane16_swap(vm[T][1], vm[T][1], false, false);
        if (g == 2) st128(rw, u32x4{s0[0], s1[0], s0[1], s1[1]}, ((s * 32 + h) * 128 + c * 32 + 16 * T + n) * 16, WS_VM);
      }
      {
        const int sa = (s - 2) & RM, sm_ = (s - 1) & RM;
        const u32x4 sv = {SENT, SENT, SENT, SENT};
        const u32 ss = nearhalf(xb + X_SB + (sa * 32 + h) * 16384, ciod) + c * 1280;
        st128(rw, sv, lane * 16, ss);
        st128(rw, sv, lane < 16 ? 1024 + lane * 16 : OOB - ss, ss);
        const int oct = 2 * (n >> 3) + (g >> 1);
        const u32 line = (((h >> 2) * 8 + (h & 1) * 4 + c) * 4 + oct) * 256 + ((h >> 1) & 1) * 128;
        st128(rw, sv, (line & 8191) + (n & 7) * 16 + (g & 1) * 8192, xb + X_OB + (sa * 8 + (line >> 13)) * 16384);
        const u32 ms = xb + X_MB + (sm_ * 2 + (jc >> 6)) * 16384;
        st128(rw, sv, lane < 16 ? (jc & 63) * 128 + (lane & 7) * 16 + (lane >> 3) * 8192 : OOB - ms, ms);
      }
      VMCNT0();
      st32(rw, 1u, lane0off(WS_CT + (CT_MF + (s * 32 + h) * 4 + c) * 4), 0);
    }
    if (w == 0) {
      if (s < NSEG - 1) {
        const u32x4* kv = (const u32x4*)(smem + L_KV + (s & 1) * 4096);
        nap(dl2);
        e2_load();
        const u32x4 vx0 = kv[128 + lane], vx1 = kv[192 + lane];
        int vflag = *(lds_vint)(smem + L_VF);
        u32x4 vmm = *(lds_vu32x4)(smem + L_VM + lane * 16);
        if (g < 2) vmm = u32x4{0, 0, 0, 0};
        const float sc = scale * l2e;
        u32x4 px;
        u32x2 pm;
        float sum;
        u32 spins = 0;
        for (;;) {
          f32x4 sx0 = (__builtin_bit_cast(f32x4, ea[0]) + __builtin_bit_cast(f32x4, ea[1])) + (__builtin_bit_cast(f32x4, ea[2]) + __builtin_bit_cast(f32x4, ea[3]));
          f32x4 sx1 = (__builtin_bit_cast(f32x4, eb[0]) + __builtin_bit_cast(f32x4, eb[1])) + (__builtin_bit_cast(f32x4, eb[2]) + __builtin_bit_cast(f32x4, eb[3]));
          f32x4 sm = (__builtin_bit_cast(f32x4, ec[0]) + __builtin_bit_cast(f32x4, ec[1])) + (__builtin_bit_cast(f32x4, ec[2]) + __builtin_bit_cast(f32x4, ec[3]));
          float mx = max3(max3(sx0[0], sx0[1], sx0[2]), max3(sx0[3], sx1[0], sx1[1]), max3(sx1[2], sx1[3], sm[0]));
          mx = max3(mx, max3(sm[1], sm[2], sm[3]), mx);
          mx = xg_max(mx);
          const float mo = mx * sc;
#pragma unroll
          for (int i = 0; i < 4; i++) {
            sx0[i] = __builtin_amdgcn_exp2f(sx0[i] * sc - mo);
            sx1[i] = __builtin_amdgcn_exp2f(sx1[i] * sc - mo);
            sm[i] = __builtin_amdgcn_exp2f(sm[i] * sc - mo);
          }
          px = u32x4{cvtpk(sx0[0], sx0[1]), cvtpk(sx0[2], sx0[3]), cvtpk(sx1[0], sx1[1]), cvtpk(sx1[2], sx1[3])};
          pm = u32x2{cvtpk(sm[0], sm[1]), cvtpk(sm[2], sm[3])};
          sum = (bflo(pm[0]) + bfhi(pm[0])) + (bflo(pm[1]) + bfhi(pm[1]));
          const bool bad = sum != sum;
          if (g < 2) { pm = u32x2{0, 0}; sum = 0.f; }
          sum += ((bflo(px[0]) + bfhi(px[0])) + (bflo(px[1]) + bfhi(px[1]))) + ((bflo(px[2]) + bfhi(px[2])) + (bflo(px[3]) + bfhi(px[3])));
          if (!__any(bad | (sum != sum))) break;
          u32 mxw = 0;
#pragma unroll
          for (int k = 0; k < 4; k++) mxw = umax(mxw, umax(max4(ea[k]), umax(max4(eb[k]), max4(ec[k]))));
          if (!__any(mxw == SENT)) break;
          if (spin_fail(rw, spins)) break;
          CBAR();
          e2_load();
        }
        adapt(dl2, D_E2, spins, s);
        while (__builtin_amdgcn_readfirstlane(vflag) != s + 1) {
          vflag = *(lds_vint)(smem + L_VF);
          vmm = *(lds_vu32x4)(smem + L_VM + lane * 16);
          if (g < 2) vmm = u32x4{0, 0, 0, 0};
        }
        const f32x4 z = {0, 0, 0, 0};
        f32x4 o0 = mfma16k16(u32x2{vmm[0], vmm[1]}, pm, mfma16(vx0, px, z));
        f32x4 o1 = mfma16k16(u32x2{vmm[2], vmm[3]}, pm, mfma16(vx1, px, z));
        const float inv = __builtin_amdgcn_rcpf(xg_sum(sum));
        f32x4 oo = n < 8 ? o0 : o1;
        const u32x2 ov = {nosent(cvtpk(oo[0] * inv, oo[1] * inv)), nosent(cvtpk(oo[2] * inv, oo[3] * inv))};
        const int oct = 2 * (n >> 3) + (g >> 1);
        const u32 line = (((h >> 2) * 8 + (h & 1) * 4 + c) * 4 + oct) * 256 + ((h >> 1) & 1) * 128;
        auto s0 = __builtin_amdgcn_permlane16_swap(ov[0], ov[0], false, false);
        auto s1 = __builtin_amdgcn_permlane16_swap(ov[1], ov[1], false, false);
        st128(rw, u32x4{s0[0], s1[0], s0[1], s1[1]}, (line & 8191) + (n & 7) * 16 + (g & 1) * 8192, xb + X_OB + ((s & RM) * 8 + (line >> 13)) * 16384);
      }
    } else if (w == 7) {
      if (s + 1 < NSEG - 1) rec_prefetch(rw, smem, s + 1, h, c, lane);
    }
    if (s == NSEG - 1) break;
    BAR();
    {
      f32x4 d;
      const u32 vo = lane * 16, so = nearhalf(xb + X_OB + ((s & RM) * 8 + w) * 16384, iod);
      u32 spins = 0;
      nap(dl3);
      for (;;) {
        CBAR();
        u32x4 of[8];
#pragma unroll
        for (int t = 0; t < 8; t++) of[t] = ld128(rw, vo, so + t * 1024);
        SCHED0();
        f32x4 e0 = {0, 0, 0, 0}, e1 = e0;
#pragma unroll
        for (int t = 0; t < 8; t += 2) { e0 = mfma16(wo[t], of[t], e0); e1 = mfma16(wo[t + 1], of[t + 1], e1); }
        d = e0 + e1;
        if (!__any(isnan4(d))) break;
        u32 mxw = 0;
#pragma unroll
        for (int t = 0; t < 8; t++) mxw = umax(mxw, max4(of[t]));
        if (!__any(mxw == SENT)) break;
        if (spin_fail(rw, spins)) break;
      }
      adapt(dl3, D_E3, spins, s);
      if ((g >> 1) == (n >> 3)) *(f32x4*)(smem + L_O + ((w * 2 + (g >> 1)) * 8 + (n & 7)) * 32 + (g & 1) * 16) = d;
    }
    BAR();
    if (w == 0) {
      const char* b = smem + L_O + (lane >> 5) * 2048 + (lane & 31) * 8;
      f32x2 a = *(const f32x2*)(b);
#pragma unroll
      for (int k = 1; k < 8; k++) a += *(const f32x2*)(b + k * 256);
      auto t0 = __builtin_amdgcn_permlane32_swap(__float_as_uint(a[0]), __float_as_uint(a[0]), false, false);
      auto t1 = __builtin_amdgcn_permlane32_swap(__float_as_uint(a[1]), __float_as_uint(a[1]), false, false);
      const u32 mv = nosent(cvtpk(__uint_as_float(t0[0]) + __uint_as_float(t0[1]), __uint_as_float(t1[0]) + __uint_as_float(t1[1])));
      const u32x4 m4 = {(u32)__builtin_amdgcn_mov_dpp((int)mv, 0x00, 0xf, 0xf, true), (u32)__builtin_amdgcn_mov_dpp((int)mv, 0x55, 0xf, 0xf, true),
                        (u32)__builtin_amdgcn_mov_dpp((int)mv, 0xAA, 0xf, 0xf, true), (u32)__builtin_amdgcn_mov_dpp((int)mv, 0xFF, 0xf, 0xf, true)};
      const u32 so = xb + X_MB + (((s + 1) & RM) * 2 + (jc >> 6)) * 16384;
      st128(rw, m4, (lane & 3) == 0 ? (jc & 63) * 128 + (lane & 31) * 4 + (lane >> 5) * 8192 : OOB - so, so);
    }
  }
}

DEVINL u64 uni64(const void* p) {
  const u64 a = (u64)p;
  return ((u64)(u32)__builtin_amdgcn_readfirstlane((int)(u32)(a >> 32)) << 32) | (u32)__builtin_amdgcn_readfirstlane((int)(u32)a);
}
DEVINL void glds16(u64 sbase, u32 voff, u32 ldsaddr) {
  u32 keep;
  asm volatile("s_mov_b32 %0, m0\n\ts_mov_b32 m0, %3\n\ts_nop 0\n\tglobal_load_lds_dwordx4 %1, %2\n\ts_mov_b32 m0, %0"
               : "=&s"(keep) : "v"(voff), "s"(sbase), "s"(ldsaddr) : "memory");
}
template <int N> DEVINL void vmcnt() { asm volatile("s_waitcnt vmcnt(%0)" ::"i"(N) : "memory"); }

template <int BM, int WM, int WN, int TI, int TJ, int NST, bool TRANS>
struct Gemm {
  static constexpr int XB = BM * 128, WB = 64 * 256, STB = XB + WB;
  static constexpr int GX = BM / 64, GW = 2, G = GX + GW;
  f32x16 acc[TI][TJ];
  template <class Dep>
  DEVINL void run(const u16* X, int ldx, const u16* W, int ldw, int nk, char* smem, Dep dep) {
    const int tid = threadIdx.x, lane = tid & 63;
    const int w = __builtin_amdgcn_readfirstlane(tid >> 6);
    const int wm = w / WN, wn = w % WN;
    const u32 lds0 = __builtin_amdgcn_readfirstlane((u32)(size_t)smem);
    u32 xo[GX], wo[GW];
#pragma unroll
    for (int i = 0; i < GX; i++) {
      const int row = (w * GX + i) * 8 + (lane >> 3);
      xo[i] = (row * ldx + (((lane & 7) ^ ((row >> 1) & 7)) * 8)) * 2;
    }
#pragma unroll
    for (int i = 0; i < GW; i++) {
      const int kr = (w * GW + i) * 4 + (lane >> 4);
      wo[i] = (kr * ldw + (((lane & 15) ^ (((kr & 3) << 2) | ((kr >> 2) & 3))) * 8)) * 2;
    }
    const u64 X64 = uni64(X), W64 = uni64(W);
    const u32 wstep = __builtin_amdgcn_readfirstlane(ldw) * 128;
    auto piece = [&](int kt, int buf, int g) {
      if (g < GX) glds16(X64 + (u32)kt * 128, xo[g < GX ? g : 0], lds0 + buf * STB + (w * GX + g) * 1024);
      else glds16(W64 + (u64)((u32)kt * wstep), wo[g >= GX ? g - GX : 0], lds0 + buf * STB + XB + (w * GW + g - GX) * 1024);
    };
    const int hh = lane >> 5, m = lane & 31;
    const int tm = TRANS ? m : (16 * ((m >> 2) & 1) + 4 * (m >> 3) + (m & 3));
    u32 xa[4];
    {
      const int row = wm * TI * 32 + tm;
#pragma unroll
      for (int s = 0; s < 4; s++) xa[s] = row * 128 + (((2 * s + hh) ^ ((row >> 1) & 7)) * 16);
    }
    u32 wa[TJ][2];
    {
      const int blk = (lane >> 4) & 1, q = (lane & 15) >> 2, pp = lane & 3;
      const int ch = TRANS ? (2 * (pp & 1) + blk) : (2 * blk + (pp >> 1));
      const int half = TRANS ? (pp >> 1) : (pp & 1);
#pragma unroll
      for (int j = 0; j < TJ; j++)
#pragma unroll
        for (int t = 0; t < 2; t++) {
          const int k = 8 * hh + 4 * t + q;
          wa[j][t] = XB + k * 256 + ((((wn * TJ + j) * 4 + ch) ^ ((q << 2) | (2 * hh + t))) * 16) + half * 8;
        }
    }
#pragma unroll
    for (int i = 0; i < TI; i++)
#pragma unroll
      for (int j = 0; j < TJ; j++)
#pragma unroll
        for (int e = 0; e < 16; e++) acc[i][j][e] = 0.f;
#pragma unroll
    for (int st = 0; st < NST - 1; st++)
#pragma unroll
      for (int g = GX; g < G; g++) piece(st, st, g);
    dep();
#pragma unroll
    for (int st = 0; st < NST - 1; st++)
#pragma unroll
      for (int g = 0; g < GX; g++) piece(st, st, g);
    int buf = 0;
    for (int kt = 0; kt < nk; kt++) {
      if (kt > 0 && kt + NST - 2 < nk) vmcnt<G * (NST - 2)>(); else vmcnt<0>();
      BAR();
      const bool more = kt + NST - 1 < nk;
      const int nbuf = buf == 0 ? NST - 1 : buf - 1;
      const char* sb = smem + buf * STB;
      u32x4 xf[2][TI], wf[2][TJ];
      auto frags = [&](int s, int d) {
#pragma unroll
        for (int j = 0; j < TJ; j++) {
          s16x4 lo = __builtin_amdgcn_ds_read_tr16_b64_v4i16((lds_s16x4)(sb + wa[j][0] + s * 4096));
          s16x4 hi = __builtin_amdgcn_ds_read_tr16_b64_v4i16((lds_s16x4)(sb + wa[j][1] + s * 4096));
          wf[d][j] = __builtin_bit_cast(u32x4, __builtin_shufflevector(lo, hi, 0, 1, 2, 3, 4, 5, 6, 7));
        }
#pragma unroll
        for (int i = 0; i < TI; i++) xf[d][i] = *(const u32x4*)(sb + xa[s] + i * 4096);
      };
      frags(0, 0);
#pragma unroll
      for (int s = 0; s < 4; s++) {
        if (s < 3) frags(s + 1, (s + 1) & 1);
        SCHED0();
        constexpr int PP = (G + 3) / 4;
#pragma unroll
        for (int i = 0; i < TI; i++)
#pragma unroll
          for (int j = 0; j < TJ; j++) {
            acc[i][j] = TRANS ? mfma32(wf[s & 1][j], xf[s & 1][i], acc[i][j]) : mfma32(xf[s & 1][i], wf[s & 1][j], acc[i][j]);
            const int q = i * TJ + j, g = s * PP + q;
            if (q < PP && g < G) { SCHED0(); if (more) piece(kt + NST - 1, nbuf, g); SCHED0(); }
          }
        SCHED0();
      }
      buf = buf == NST - 1 ? 0 : buf + 1;
    }
    BAR();
  }
};

DEVINL float mish(float hv) {
  const float u = __expf(-fabsf(hv));
  const float num = hv > 0.f ? 1.f + 2.f * u : u * (u + 2.f);
  const float den = hv > 0.f ? num + 2.f * u * u : num + 2.f;
  return hv * num / den;
}

DEVINL void job_done(rsrc_t rw, int word) {
  VMCNT0();
  BAR();
  if (uni(threadIdx.x >> 6) == 0) __builtin_amdgcn_raw_ptr_buffer_atomic_add_i32(1, rw, lane0off(WS_CT + word * 4), 0, SC1);
}
DEVINL void job_wait(rsrc_t rw, int word, u32 target, int word2, u32 target2) {
  if (uni(threadIdx.x >> 6) == 0) {
    wait_ctr(rw, word, target);
    if (word2 >= 0) wait_ctr(rw, word2, target2);
    __builtin_amdgcn_fence(__ATOMIC_ACQUIRE, "agent");
    VMCNT0();
  }
  BAR();
}

template <class Dep>
DEVINL void job_proj(const Params& p, rsrc_t rw, char* smem, int kind, int rb, int ct, Dep dep) {
  const int tid = threadIdx.x, lane = tid & 63, w = tid >> 6, wm = w >> 1, wn = w & 1, hh = lane >> 5, m = lane & 31;
  const u16* X = (const u16*)(p.ws + WS_XN) + (size_t)rb * 256 * D_;
  if (kind == 1) {
    Gemm<256, 4, 2, 2, 2, 3, false> gm;
    gm.run(X, D_, p.wv + ct * 128, HD, 16, smem, dep);
#pragma unroll
    for (int i = 0; i < 2; i++)
#pragma unroll
      for (int j = 0; j < 2; j++) {
        const int seg = rb * 8 + wm * 2 + i, e = wn * 64 + j * 32 + m;
        const u32 o = (((seg * 32 + ct) * 128 + e) * 32 + 16 * hh) * 2;
        const f32x16 a = gm.acc[i][j];
        st128(rw, u32x4{cvtpk(a[0], a[1]), cvtpk(a[2], a[3]), cvtpk(a[4], a[5]), cvtpk(a[6], a[7])}, o, WS_VT);
        st128(rw, u32x4{cvtpk(a[8], a[9]), cvtpk(a[10], a[11]), cvtpk(a[12], a[13]), cvtpk(a[14], a[15])}, o + 16, WS_VT);
      }
    job_done(rw, CT_KV + rb * 32);
  } else {
    Gemm<256, 4, 2, 2, 2, 3, true> gm;
    const u16* W = kind == 0 ? p.wk : kind == 2 ? p.wq : p.w1;
    const int ldw = kind == 3 ? F_ : HD;
    gm.run(X, D_, W + ct * 128, ldw, 16, smem, dep);
    const u32 base = kind == 0 ? WS_KX : kind == 2 ? WS_QX : WS_ACT;
#pragma unroll
    for (int j = 0; j < 2; j++) {
      const int n0 = ct * 128 + wn * 64 + j * 32 + 16 * hh;
      float bias[16];
      if (kind == 3) {
        const u32x4 b0 = *(const u32x4*)(p.b1 + n0), b1 = *(const u32x4*)(p.b1 + n0 + 8);
#pragma unroll
        for (int e = 0; e < 4; e++) { bias[2 * e] = bflo(b0[e]); bias[2 * e + 1] = bfhi(b0[e]); bias[8 + 2 * e] = bflo(b1[e]); bias[9 + 2 * e] = bfhi(b1[e]); }
      }
#pragma unroll
      for (int i = 0; i < 2; i++) {
        const int tok = rb * 256 + wm * 64 + i * 32 + m;
        f32x16 a = gm.acc[i][j];
        if (kind == 3) {
#pragma unroll
          for (int e = 0; e < 16; e++) a[e] = mish(a[e] + bias[e]);
        }
        const u32 o = (tok * ldw + n0) * 2;
        st128(rw, u32x4{cvtpk(a[0], a[1]), cvtpk(a[2], a[3]), cvtpk(a[4], a[5]), cvtpk(a[6], a[7])}, o, base);
        st128(rw, u32x4{cvtpk(a[8], a[9]), cvtpk(a[10], a[11]), cvtpk(a[12], a[13]), cvtpk(a[14], a[15])}, o + 16, base);
      }
    }
    job_done(rw, (kind == 0 ? CT_KV : kind == 2 ? CT_Q : CT_F1) + rb * 32);
  }
}

template <class Dep>
DEVINL void job_kv0(const Params& p, rsrc_t rw, char* smem, int kind, int rb, int ct, Dep dep) {
  const int tid = threadIdx.x, lane = tid & 63, w = tid >> 6, wm = w >> 2, wn = w & 3, hh = lane >> 5, m = lane & 31;
  const u16* X = (const u16*)(p.ws + WS_XN) + (size_t)rb * 64 * D_;
  f32x16 a;
  u32 o;
  if (kind == 1) {
    Gemm<64, 2, 4, 1, 1, 6, false> gm;
    gm.run(X, D_, p.wv + ct * 128, HD, 16, smem, dep);
    a = gm.acc[0][0];
    o = WS_VT + ((((rb * 2 + wm) * 32 + ct) * 128 + wn * 32 + m) * 32 + 16 * hh) * 2;
  } else {
    Gemm<64, 2, 4, 1, 1, 6, true> gm;
    gm.run(X, D_, p.wk + ct * 128, HD, 16, smem, dep);
    a = gm.acc[0][0];
    o = WS_KX + ((rb * 64 + wm * 32 + m) * HD + ct * 128 + wn * 32 + 16 * hh) * 2;
  }
  st128(rw, u32x4{cvtpk(a[0], a[1]), cvtpk(a[2], a[3]), cvtpk(a[4], a[5]), cvtpk(a[6], a[7])}, o, 0);
  st128(rw, u32x4{cvtpk(a[8], a[9]), cvtpk(a[10], a[11]), cvtpk(a[12], a[13]), cvtpk(a[14], a[15])}, o + 16, 0);
  job_done(rw, CT_KV0 + rb * 32);
}

DEVINL void job_ffn2(const Params& p, rsrc_t rw, char* smem, int rb, int ct) {
  const int tid = threadIdx.x, lane = tid & 63, w = tid >> 6, wm = w >> 1, wn = w & 1, hh = lane >> 5, m = lane & 31;
  Gemm<256, 4, 2, 2, 2, 3, true> gm;
  gm.run((const u16*)(p.ws + WS_ACT) + (size_t)rb * 256 * F_, F_, p.w2 + ct * 128, D_, F_ / 64, smem,
         [&]() { job_wait(rw, CT_F1 + rb * 32, 28, -1, 0); });
#pragma unroll
  for (int i = 0; i < 2; i++)
#pragma unroll
    for (int j = 0; j < 2; j++) {
      const int tok = rb * 256 + wm * 64 + i * 32 + m, n0 = ct * 128 + wn * 64 + j * 32 + 16 * hh;
      const u32x4 x0 = *(const u32x4*)(p.x + tok * D_ + n0), x1 = *(const u32x4*)(p.x + tok * D_ + n0 + 8);
      const f32x16 a = gm.acc[i][j];
      const u32 o = (tok * D_ + n0) * 4;
#pragma unroll
      for (int q = 0; q < 4; q++) {
        const u32 xa = q < 2 ? x0[2 * q] : x1[2 * q - 4], xb = q < 2 ? x0[2 * q + 1] : x1[2 * q - 3];
        const f32x4 v = {a[4 * q] + bflo(xa), a[4 * q + 1] + bfhi(xa), a[4 * q + 2] + bflo(xb), a[4 * q + 3] + bfhi(xb)};
        st128(rw, __builtin_bit_cast(u32x4, v), o + q * 16, WS_Y);
      }
    }
  job_done(rw, CT_F2 + rb * 32);
}

DEVINL void job_wo(const Params& p, rsrc_t rw, char* smem, int rb, int ct) {
  const int tid = threadIdx.x, lane = tid & 63, w = tid >> 6, wm = w >> 2, wn = w & 3, hh = lane >> 5, m = lane & 31;
  Gemm<64, 2, 4, 1, 1, 6, true> gm;
  gm.run((const u16*)(p.ws + WS_OX) + (size_t)rb * 64 * HD, HD, p.wo + ct * 128, D_, HD / 64, smem,
         [&]() { job_wait(rw, CT_OX + rb * 32, 8, CT_F2 + (rb >> 2) * 32, 8); });
  const int tok = rb * 64 + wm * 32 + m, n0 = ct * 128 + wn * 32 + 16 * hh;
  const f32x4* y = (const f32x4*)(p.ws + WS_Y + (size_t)(tok * D_ + n0) * 4);
  const f32x16 a = gm.acc[0][0];
  u32 o[8];
#pragma unroll
  for (int q = 0; q < 4; q++) {
    const f32x4 yv = y[q];
    o[2 * q] = cvtpk(a[4 * q] + yv[0], a[4 * q + 1] + yv[1]);
    o[2 * q + 1] = cvtpk(a[4 * q + 2] + yv[2], a[4 * q + 3] + yv[3]);
  }
  u32x4* dst = (u32x4*)(p.out + tok * D_ + n0);
  dst[0] = u32x4{o[0], o[1], o[2], o[3]};
  dst[1] = u32x4{o[4], o[5], o[6], o[7]};
}

DEVINL void job_wo_sk(const Params& p, rsrc_t rw, char* smem, int rb, int ct, int ks) {
  const int tid = threadIdx.x, lane = tid & 63, w = tid >> 6, wm = w >> 2, wn = w & 3, hh = lane >> 5, m = lane & 31;
  const int g = (rb - (32 - NSK)) * 8 + ct;
  Gemm<64, 2, 4, 1, 1, 6, true> gm;
  gm.run((const u16*)(p.ws + WS_OX) + (size_t)rb * 64 * HD + ks * 512, HD, p.wo + (size_t)ks * 512 * D_ + ct * 128, D_, 8, smem,
         [&]() { job_wait(rw, CT_OX + rb * 32, 8, CT_F2 + (rb >> 2) * 32, 8); });
  const f32x16 a = gm.acc[0][0];
  const u32 so = WS_SL + (g * 8 + ks) * 32768 + ((wm * 32 + m) * 128 + wn * 32 + 16 * hh) * 4;
#pragma unroll
  for (int q = 0; q < 4; q++) st128(rw, __builtin_bit_cast(u32x4, f32x4{a[4 * q], a[4 * q + 1], a[4 * q + 2], a[4 * q + 3]}), so + q * 16, 0);
  job_done(rw, CT_SK + g * 32);
  if (uni(w) == 0) { wait_ctr(rw, CT_SK + g * 32, 8); }
  BAR();
  if (tid < 256) {
    const int row = ks * 8 + (tid >> 5), col = (tid & 31) * 4;
    const int tok = rb * 64 + row;
    f32x4 v = __builtin_bit_cast(f32x4, ld128(rw, (tok * D_ + ct * 128 + col) * 4, WS_Y));
#pragma unroll
    for (int k = 0; k < 8; k++) v += __builtin_bit_cast(f32x4, ld128(rw, (row * 128 + col) * 4, WS_SL + (g * 8 + k) * 32768));
    *(u32x2*)(p.out + tok * D_ + ct * 128 + col) = u32x2{cvtpk(v[0], v[1]), cvtpk(v[2], v[3])};
  }
}

DEVINL void att_wave(rsrc_t rw, int s, int h, int lane) {
  const int g = lane >> 4, n = lane & 15;
  f32x4 sc2[2][3];
#pragma unroll
  for (int a = 0; a < 2; a++)
#pragma unroll
    for (int b = 0; b < 3; b++) sc2[a][b] = f32x4{0, 0, 0, 0};
  u32x4 q0[4], q1[4];
#pragma unroll
  for (int t = 0; t < 4; t++) {
    const u32 col = (h * DH + 32 * t + 8 * g) * 2;
    q0[t] = ld128(rw, (32 * s + n) * HD * 2 + col, WS_QX); q1[t] = ld128(rw, (32 * s + 16 + n) * HD * 2 + col, WS_QX);
    const u32x4 k0 = ld128(rw, (32 * s + n) * HD * 2 + col, WS_KX), k1 = ld128(rw, (32 * s + 16 + n) * HD * 2 + col, WS_KX);
    sc2[0][0] = mfma16(k0, q0[t], sc2[0][0]); sc2[0][1] = mfma16(k1, q0[t], sc2[0][1]);
    sc2[1][0] = mfma16(k0, q1[t], sc2[1][0]); sc2[1][1] = mfma16(k1, q1[t], sc2[1][1]);
  }
  u32x4 vx[8];
#pragma unroll
  for (int T = 0; T < 8; T++) {
    const int e = 32 * (T >> 1) + 8 * (n >> 2) + 4 * (T & 1) + (n & 3);
    const u32 vo = (((s * 32 + h) * 128 + e) * 32 + 4 * g) * 2;
    const u32x2 lo = ld64(rw, vo, WS_VT), hi = ld64(rw, vo + 32, WS_VT);
    vx[T] = u32x4{lo[0], lo[1], hi[0], hi[1]};
  }
  asm volatile("" : "+v"(vx[0]), "+v"(vx[1]), "+v"(vx[2]), "+v"(vx[3]), "+v"(vx[4]), "+v"(vx[5]), "+v"(vx[6]), "+v"(vx[7]));
  if (uni(threadIdx.x >> 6) == 0) {
    u32 spins = 0;
    for (;;) {
      CBAR();
      const u32 f = ld32(rw, WS_CT + (CT_MF + (s * 32 + (h & ~7)) * 4 + (lane & 31)) * 4, 0);
      if (__all(f != 0)) break;
      if ((++spins & 255) == 0) {
        const u32 t = uni(ld32(rw, WS_CT + CT_TMO * 4, 0));
        if (t != 0 || spins > SPIN_CAP) { st32(rw, 1u, lane0off(WS_CT + CT_TMO * 4), 0); break; }
      }
      __builtin_amdgcn_s_sleep(3);
    }
  }
  BAR();
  u32x4 km[4];
  u32x2 vm[8];
#pragma unroll
  for (int t = 0; t < 4; t++) km[t] = ld128(rw, (((s * 32 + h) * 8 + (n & 7)) * 128 + 32 * t + 8 * g) * 2, WS_KM);
#pragma unroll
  for (int T = 0; T < 8; T++) {
    const int e = 32 * (T >> 1) + 8 * (n >> 2) + 4 * (T & 1) + (n & 3);
    vm[T] = ld64(rw, ((s * 32 + h) * 128 + e) * 16 + (g & 1) * 8, WS_VM);
  }
  SCHED0();
#pragma unroll
  for (int t = 0; t < 4; t++) { sc2[0][2] = mfma16(km[t], q0[t], sc2[0][2]); sc2[1][2] = mfma16(km[t], q1[t], sc2[1][2]); }
  const float sc = 0.08838834764831845f * 1.4426950408889634f;
  u32x4 px[2]; u32x2 pm[2];
#pragma unroll
  for (int a = 0; a < 2; a++) {
    const int qq = 16 * a + n;
    const float ninf = -__builtin_inff();
    float v[12];
#pragma unroll
    for (int i = 0; i < 4; i++) {
      v[i] = (4 * g + i <= qq) ? sc2[a][0][i] : ninf;
      v[4 + i] = (16 + 4 * g + i <= qq) ? sc2[a][1][i] : ninf;
      v[8 + i] = g < 2 ? sc2[a][2][i] : ninf;
    }
    float mx = max3(max3(v[0], v[1], v[2]), max3(v[3], v[4], v[5]), max3(v[6], v[7], v[8]));
    mx = xg_max(max3(mx, max3(v[9], v[10], v[11]), mx));
    const float mo = mx * sc;
    float sum = 0.f;
#pragma unroll
    for (int i = 0; i < 12; i++) { v[i] = __builtin_amdgcn_exp2f(v[i] * sc - mo); sum += v[i]; }
    sum = xg_sum(sum);
    const float inv = 1.0f / sum;
    px[a] = u32x4{cvtpk(v[0] * inv, v[1] * inv), cvtpk(v[2] * inv, v[3] * inv), cvtpk(v[4] * inv, v[5] * inv), cvtpk(v[6] * inv, v[7] * inv)};
    pm[a] = u32x2{cvtpk(v[8] * inv, v[9] * inv), cvtpk(v[10] * inv, v[11] * inv)};
  }
#pragma unroll
  for (int u = 0; u < 4; u++) {
    f32x4 o[2][2];
#pragma unroll
    for (int T = 0; T < 2; T++) {
      const u32x2 vmz = g >= 2 ? u32x2{0, 0} : vm[2 * u + T];
      const f32x4 z = {0, 0, 0, 0};
      o[0][T] = mfma16k16(vmz, pm[0], mfma16(vx[2 * u + T], px[0], z));
      o[1][T] = mfma16k16(vmz, pm[1], mfma16(vx[2 * u + T], px[1], z));
    }
#pragma unroll
    for (int a = 0; a < 2; a++) {
      const u32x4 ov = {cvtpk(o[a][0][0], o[a][0][1]), cvtpk(o[a][0][2], o[a][0][3]), cvtpk(o[a][1][0], o[a][1][1]), cvtpk(o[a][1][2], o[a][1][3])};
      st128(rw, ov, ((32 * s + 16 * a + n) * HD + h * DH + 32 * u + 8 * g) * 2, WS_OX);
    }
  }
}

constexpr int NJ_KV0 = 32, NJ_KV = 56, NJ_F1 = 32, NJ_Q = 32, NJ_F2 = 8, NJ_TAIL = (32 - NSK) * 2, NJ_SK = NSK * 9;
constexpr int J_KV = NJ_KV0, J_F1 = J_KV + NJ_KV, J_Q = J_F1 + NJ_F1, J_F2 = J_Q + NJ_Q, J_TAIL = J_F2 + NJ_F2, J_SK = J_TAIL + NJ_TAIL,
              J_END = J_SK + NJ_SK;

DEVINL void job_att(const Params& p, rsrc_t rw, int rb, int k) {
  const int s = rb * 2 + (k >> 2), h = (k & 3) * 8 + (threadIdx.x >> 6);
  if (s < 8) job_wait(rw, CT_KV0 + (s >> 1) * 32, 64, CT_Q, 32);
  else job_wait(rw, CT_KV + (s >> 3) * 32, 64, CT_Q + (s >> 3) * 32, 32);
  att_wave(rw, s, h, threadIdx.x & 63);
  job_done(rw, CT_OX + rb * 32);
}

DEVINL void worker_role(const Params& p, char* smem) {
  const rsrc_t rw = mkrsrc(p.ws, WS_END);
  int* sjob = (int*)(smem + 147456);
  bool prep_seen = false;
  for (;;) {
    if (uni(threadIdx.x >> 6) == 0) {
      const int t = __builtin_amdgcn_raw_ptr_buffer_atomic_add_i32(1, rw, lane0off(WS_CT + CT_TICKET * 4), 0, SC1);
      *sjob = __builtin_amdgcn_readfirstlane(t);
    }
    BAR();
    const int t = __builtin_amdgcn_readfirstlane(*sjob);
    const int q = t & 7, j = t >> 3;
    BAR();
    if (j >= J_END) break;
    auto prep_dep = [&]() { if (!prep_seen) { job_wait(rw, CT_PREP, 128, -1, 0); prep_seen = true; } };
    if (j < J_KV) {
      job_kv0(p, rw, smem, (j >> 2) & 1, j >> 3, (j & 3) * 8 + q, prep_dep);
    } else if (j < J_F2) {
      int kind, rb, ct;
      if (j < J_F1) { const int i = j - J_KV; rb = 1 + (i >> 3); ct = (i & 3) * 8 + q; kind = (i >> 2) & 1; }
      else if (j < J_Q) { const int i = j - J_F1; rb = i >> 2; ct = (i & 3) * 8 + q; kind = 3; }
      else { const int i = j - J_Q; rb = i >> 2; ct = (i & 3) * 8 + q; kind = 2; }
      if (kind != 3 || ct < 28) job_proj(p, rw, smem, kind, rb, ct, prep_dep);
    } else if (j < J_TAIL) {
      job_ffn2(p, rw, smem, j - J_F2, q);
    } else if (j < J_SK) {
      const int i = j - J_TAIL;
      if ((i & 1) == 0) job_att(p, rw, i >> 1, q);
      else job_wo(p, rw, smem, i >> 1, q);
    } else {
      const int i = j - J_SK, rb = 32 - NSK + i / 9, k = i % 9;
      if (k == 0) job_att(p, rw, rb, q);
      else job_wo_sk(p, rw, smem, rb, q, k - 1);
    }
  }
}

constexpr int LDS_BYTES = 147456 + 64;

__global__ __launch_bounds__(512, 2) void main_kernel(Params p) {
  extern __shared__ __attribute__((aligned(16))) char smem[];
  if (blockIdx.x < 128) {
    rec_role(p, smem, blockIdx.x);
    __syncthreads();
  } else {
    const rsrc_t rw = mkrsrc(p.ws, WS_END);
    prep_role(p, rw, blockIdx.x - 128);
    job_done(rw, CT_PREP);
  }
  worker_role(p, smem);
}

extern "C" void kernel_launch(void* const* d_in, const int* in_sizes, int n_in, void* d_out, int out_size, void* d_ws,
                              size_t ws_size, hipStream_t stream) {
  Params p;
  p.x = (const u16*)d_in[0];
  p.mem0 = (const u16*)d_in[1];
  p.wq = (const u16*)d_in[2];
  p.wk = (const u16*)d_in[3];
  p.wv = (const u16*)d_in[4];
  p.wo = (const u16*)d_in[5];
  p.w1 = (const u16*)d_in[8];
  p.b1 = (const u16*)d_in[9];
  p.w2 = (const u16*)d_in[10];
  p.out = (u16*)d_out;
  p.ws = (char*)d_ws;
  hipFuncSetAttribute((const void*)main_kernel, hipFuncAttributeMaxDynamicSharedMemorySize, LDS_BYTES);
  hipMemsetAsync(p.ws + WS_CT, 0, CT_BYTES, stream);
  main_kernel<<<256, 512, LDS_BYTES, stream>>>(p);
}
```
